```python
import torch

def _softmax(x, axis=-1):
    return torch.softmax(x, dim=axis)

def _gelu(x):
    return torch.nn.functional.gelu(x, approximate='tanh')

def _silu(x):
    return torch.nn.functional.silu(x)

def _sigmoid(x):
    return torch.sigmoid(x)

def _layernorm(x, axis=-1, eps=1e-05):
    m = x.mean(dim=axis, keepdim=True)
    v = x.var(dim=axis, keepdim=True, unbiased=False)
    return (x - m) / torch.sqrt(v + eps)

def _scatter_add(tgt, idx, src, axis=0):
    return tgt.clone().index_add_(axis, idx, src)

def _segment_sum(src, seg, n):
    out = torch.zeros((n,) + src.shape[1:], dtype=src.dtype)
    return out.index_add_(0, seg, src)

def _pad(x, pw):
    flat = tuple((v for pair in reversed(pw) for v in pair))
    return torch.nn.functional.pad(x, flat)
kernel_name = 'gather_scatter__tensor_product_equiv_equiformer__graph'

def setup_inputs():

    _w = lambda a: (lambda t: t.to(torch.bfloat16) if t.is_floating_point() else t)(torch.as_tensor(a))
    N, deg, C, De = (10000, 64, 64, max(16, 1))
    E = N * deg
    src = (lambda lo, hi, *s: torch.randint(lo, hi, s))(0, N, E).to(torch.int64)
    dst = (lambda lo, hi, *s: torch.randint(lo, hi, s))(0, N, E).to(torch.int64)
    rvec = (lambda *s: torch.randn(*s, dtype=torch.bfloat16) * 0.05)(E, 3).to(float)
    rn = (torch.linalg.norm(rvec, dim=-1, keepdim=True) + 1e-06).to(float)
    rhat = (rvec / rn).to(float)
    eps = torch.zeros((3, 3, 3), dtype=float)
    eps[0, 1, 2] = eps[1, 2, 0] = eps[2, 0, 1] = 1.0
    eps[0, 2, 1] = eps[2, 1, 0] = eps[1, 0, 2] = -1.0
    return {'x': _w((lambda *s: torch.randn(*s, dtype=torch.bfloat16) * 0.2)(N, 4 * C).to(float)), 'rhat': _w(rhat), 'e_attr': _w((lambda *s: torch.randn(*s, dtype=torch.bfloat16) * 0.2)(E, De).to(float)), 'src': _w(src), 'dst': _w(dst), 'w_rad': _w((lambda *s: torch.randn(*s, dtype=torch.bfloat16) * 0.2)(De, 5 * C).to(float)), 'w_si0': _w((lambda *s: torch.randn(*s, dtype=torch.bfloat16) * 0.2)(2 * C, C).to(float)), 'w_si1': _w((lambda *s: torch.randn(*s, dtype=torch.bfloat16) * 0.05)(3 * C, C).to(float)), 'cg_eps': _w(eps)}

def reference(x, rhat, e_attr, src, dst, w_rad, w_si0, w_si1, cg_eps):
    N = x.shape[0]
    C = w_si0.shape[1]
    E = rhat.shape[0]
    x0 = x[:, :C].clone()
    x1 = x[:, C:].reshape(N, 3, C)
    R = (e_attr @ w_rad).reshape(E, 5, C)
    f0 = x0[src]
    f1 = x1[src]
    Y1 = rhat
    p00_0 = f0 * R[:, 0, :]
    p11_0 = torch.einsum('ejc,ej->ec', f1, Y1) * R[:, 1, :]
    p01_1 = torch.einsum('ec,ej->ejc', f0, Y1) * R[:, 2, :][:, None, :]
    p10_1 = f1 * R[:, 3, :][:, None, :]
    p11_1 = torch.einsum('ijk,ekc,ej->eic', cg_eps, f1, Y1) * R[:, 4, :][:, None, :]
    m0 = torch.concatenate([p00_0, p11_0], axis=-1) @ w_si0
    m1 = torch.einsum('ejp,pc->ejc', torch.concatenate([p01_1, p10_1, p11_1], axis=-1), w_si1)
    agg0 = _segment_sum(m0, dst, N)
    agg1 = _segment_sum(m1.reshape(E, 3 * C), dst, N).reshape(N, 3, C)
    gate = _sigmoid(agg0)
    out1 = agg1 * gate[:, None, :]
    return torch.concatenate([agg0, out1.reshape(N, 3 * C)], axis=-1)
```

```cpp
#include <hip/hip_runtime.h>
#include <hip/hip_bf16.h>
#include <stdint.h>

typedef unsigned short u16;
__device__ __forceinline__ float bf2f(u16 v) { return __uint_as_float(((unsigned)v) << 16); }
__device__ __forceinline__ u16 f2bf(float f) { __hip_bfloat16 b = __float2bfloat16(f); return *(u16*)&b; }

__global__ void k_hist(const int* dst, int E, int* cnt) {
  int e = blockIdx.x * blockDim.x + threadIdx.x;
  if (e < E) atomicAdd(&cnt[dst[e]], 1);
}
__global__ void k_scan(const int* cnt, int N, int* off, int* cur) {
  __shared__ int s[1024];
  int t = threadIdx.x;
  int per = (N + 1023) / 1024;
  int sum = 0;
  for (int i = 0; i < per; ++i) { int n = t * per + i; if (n < N) sum += cnt[n]; }
  s[t] = sum; __syncthreads();
  for (int d = 1; d < 1024; d <<= 1) { int v = t >= d ? s[t - d] : 0; __syncthreads(); s[t] += v; __syncthreads(); }
  int base = s[t] - sum;
  for (int i = 0; i < per; ++i) { int n = t * per + i; if (n < N) { off[n] = base; cur[n] = base; base += cnt[n]; } }
  if (t == 1023) off[N] = s[1023];
}
__global__ void k_fill(const int* dst, int E, int* cur, int* perm) {
  int e = blockIdx.x * blockDim.x + threadIdx.x;
  if (e < E) { int p = atomicAdd(&cur[dst[e]], 1); perm[p] = e; }
}
__global__ __launch_bounds__(64) void k_node(const u16* x, const u16* rhat, const u16* ea, const int* src, const int* off, const int* perm,
                       const u16* wrad, const u16* wsi0, const u16* wsi1, const u16* eps, u16* out) {
  int n = blockIdx.x, c = threadIdx.x;
  __shared__ float A[11 * 64];
  float w[5][16];
  for (int p = 0; p < 5; ++p) for (int d = 0; d < 16; ++d) w[p][d] = bf2f(wrad[d * 320 + p * 64 + c]);
  float ep[27];
  for (int i = 0; i < 27; ++i) ep[i] = bf2f(eps[i]);
  float acc[11];
  for (int i = 0; i < 11; ++i) acc[i] = 0.f;
  for (int q = off[n]; q < off[n + 1]; ++q) {
    int e = perm[q]; int s = src[e];
    float R[5] = {0, 0, 0, 0, 0};
    for (int d = 0; d < 16; ++d) { float a = bf2f(ea[e * 16 + d]); for (int p = 0; p < 5; ++p) R[p] += a * w[p][d]; }
    float Y[3]; for (int j = 0; j < 3; ++j) Y[j] = bf2f(rhat[e * 3 + j]);
    float f0 = bf2f(x[s * 256 + c]); float f1[3]; for (int j = 0; j < 3; ++j) f1[j] = bf2f(x[s * 256 + 64 + j * 64 + c]);
    acc[0] += f0 * R[0];
    acc[1] += (f1[0] * Y[0] + f1[1] * Y[1] + f1[2] * Y[2]) * R[1];
    for (int j = 0; j < 3; ++j) {
      acc[2 + j] += f0 * Y[j] * R[2];
      acc[5 + j] += f1[j] * R[3];
      float cr = 0.f;
      for (int jj = 0; jj < 3; ++jj) for (int k = 0; k < 3; ++k) cr += ep[j * 9 + jj * 3 + k] * f1[k] * Y[jj];
      acc[8 + j] += cr * R[4];
    }
  }
  for (int i = 0; i < 11; ++i) A[i * 64 + c] = acc[i];
  __syncthreads();
  float a0 = 0.f;
  for (int k = 0; k < 128; ++k) a0 += A[k] * bf2f(wsi0[k * 64 + c]);
  float g = 1.f / (1.f + __expf(-a0));
  out[n * 256 + c] = f2bf(a0);
  for (int j = 0; j < 3; ++j) {
    float a1 = 0.f;
    for (int k = 0; k < 64; ++k) {
      a1 += A[(2 + j) * 64 + k] * bf2f(wsi1[k * 64 + c]);
      a1 += A[(5 + j) * 64 + k] * bf2f(wsi1[(64 + k) * 64 + c]);
      a1 += A[(8 + j) * 64 + k] * bf2f(wsi1[(128 + k) * 64 + c]);
    }
    out[n * 256 + 64 + j * 64 + c] = f2bf(a1 * g);
  }
}

extern "C" void kernel_launch(void* const* d_in, const int* in_sizes, int n_in,
                              void* d_out, int out_size, void* d_ws, size_t ws_size,
                              hipStream_t stream) {
  const u16* x = (const u16*)d_in[0]; const u16* rhat = (const u16*)d_in[1]; const u16* ea = (const u16*)d_in[2];
  const int* src = (const int*)d_in[3]; const int* dst = (const int*)d_in[4];
  const u16* wrad = (const u16*)d_in[5]; const u16* wsi0 = (const u16*)d_in[6]; const u16* wsi1 = (const u16*)d_in[7]; const u16* eps = (const u16*)d_in[8];
  int N = in_sizes[0] / 256, E = in_sizes[3];
  int* cnt = (int*)d_ws; int* off = cnt + 16384; int* cur = off + 16384; int* perm = cur + 16384;
  hipMemsetAsync(cnt, 0, 16384 * 4, stream);
  k_hist<<<(E + 255) / 256, 256, 0, stream>>>(dst, E, cnt);
  k_scan<<<1, 1024, 0, stream>>>(cnt, N, off, cur);
  k_fill<<<(E + 255) / 256, 256, 0, stream>>>(dst, E, cur, perm);
  k_node<<<N, 64, 0, stream>>>(x, rhat, ea, src, off, perm, wrad, wsi0, wsi1, eps, (u16*)d_out);
}
```

```cpp
#include <hip/hip_runtime.h>
#include <stdint.h>

typedef unsigned short u16;
typedef unsigned u32;
typedef __attribute__((ext_vector_type(2))) u32 u32x2;
typedef __attribute__((ext_vector_type(3))) u32 u32x3;
typedef __attribute__((ext_vector_type(4))) u32 u32x4;
typedef __attribute__((ext_vector_type(8))) u32 u32x8;
typedef __attribute__((ext_vector_type(4))) float f32x4;
typedef __attribute__((ext_vector_type(16))) float f32x16;
typedef __attribute__((ext_vector_type(8))) __bf16 bf16x8;
typedef __attribute__((ext_vector_type(16))) __bf16 bf16x16;
#define LDSP(T, a) ((__attribute__((address_space(3))) T*)(uintptr_t)(u32)(a))

#define AINL __attribute__((always_inline))
#if defined(__HIP_DEVICE_COMPILE__)
#define NOPK __attribute__((target("no-packed-fp32-ops")))
#else
#define NOPK
#endif

constexpr int NBK = 256;
constexpr int CHCAP = 2560;
constexpr int TABS = 264;
constexpr int GRP = 368;
constexpr int MINGRP = 7;
constexpr int RCAP = 3072;

constexpr int L_W = 0;
constexpr int L_CNT = 20480;
constexpr int L_WT = L_CNT + 2048;
constexpr int L_ONE = L_WT + 32;
constexpr int L_NI = L_ONE + 32;
constexpr int L_ZG = L_NI + 256;
constexpr int L_REG = L_ZG + GRP;
constexpr int L_TOTAL = 163840;
constexpr int GCAP = (L_TOTAL - L_REG) / GRP;

__device__ __forceinline__ float bf2f(u32 v) { return __uint_as_float(v << 16); }
typedef __attribute__((ext_vector_type(2))) __bf16 bf16x2;
typedef __attribute__((ext_vector_type(2))) float f32x2;
__device__ __forceinline__ u32 cvtpk(float lo, float hi) { f32x2 v = {lo, hi}; return __builtin_bit_cast(u32, __builtin_convertvector(v, bf16x2)); }
__device__ __forceinline__ void bar() { asm volatile("s_waitcnt lgkmcnt(0)\n\ts_barrier" ::: "memory"); }
__device__ __forceinline__ int shfl(int v, int l) { return __builtin_amdgcn_ds_bpermute(l << 2, v); }
__device__ __forceinline__ int opaque(int v) { asm volatile("" : "+s"(v)); return v; }
__device__ __forceinline__ int wave_incl_scan(int v, int lane) {
#pragma unroll
  for (int d = 1; d < 64; d <<= 1) { int o = __shfl_up(v, d); if (lane >= d) v += o; }
  return v;
}

__global__ __launch_bounds__(1024) void k_bin(const u16* __restrict__ rhat, const u32x4* __restrict__ ea, const int* __restrict__ src,
                                              const int* __restrict__ dst, const u16* __restrict__ wsi0, const u16* __restrict__ wsi1,
                                              const u16* __restrict__ eps, u32x4* __restrict__ recA, u32* __restrict__ recM,
                                              u16* __restrict__ tab, u16* __restrict__ wfrag, float* __restrict__ epsf,
                                              int E, int chunk, u32 M) {
  __shared__ u32x4 sA[CHCAP * 2];
  __shared__ __attribute__((aligned(16))) u32 sM[CHCAP * 3];
  __shared__ __attribute__((aligned(16))) u32 cnt[16 * NBK];
  __shared__ u32 wt[4];
  const int t = threadIdx.x, lane = t & 63, w = t >> 6, c = blockIdx.x;
  const int e0 = min(c * chunk, E - 1);
  const int n = max(0, min(chunk, E - c * chunk));
  int dv[3], sv[3]; u32 y[3][3]; u32x4 av[3][2];
#pragma unroll
  for (int k = 0; k < 3; ++k) {
    int i = t + k * 1024; int e = e0 + (i < n ? i : 0);
    dv[k] = __builtin_nontemporal_load(dst + e);
  }
#pragma unroll
  for (int k = 0; k < 3; ++k) {
    int i = t + k * 1024; int e = e0 + (i < n ? i : 0);
    sv[k] = __builtin_nontemporal_load(src + e);
#pragma unroll
    for (int j = 0; j < 3; ++j) y[k][j] = __builtin_nontemporal_load(rhat + (size_t)e * 3 + j);
#pragma unroll
    for (int j = 0; j < 2; ++j) {
      int q = (w * 64 + k * 1024) * 2 + j * 64 + lane;
      av[k][j] = __builtin_nontemporal_load(ea + (size_t)e0 * 2 + (q < 2 * n ? q : 0));
    }
  }
  ((u32x4*)cnt)[t] = u32x4{0, 0, 0, 0};
  if (t < 80) {
    int idx = c * 80 + t;
    int j = idx & 7, l = (idx >> 3) & 63, nt = (idx >> 9) & 1, u = (idx >> 10) & 1, ks = (idx >> 11) & 1, blk = idx >> 12;
    int rho = l & 15, q = l >> 4;
    int k = ks * 32 + q * 8 + j, ch = 32 * u + 8 * (rho >> 2) + 4 * nt + (rho & 3);
    wfrag[idx] = blk < 2 ? wsi0[(blk * 64 + k) * 64 + ch] : wsi1[((blk - 2) * 64 + k) * 64 + ch];
  }
  if (c == 0 && t >= 128 && t < 128 + 27) epsf[t - 128] = bf2f(eps[t - 128]);
  bar();
  int bk[3], rk[3];
#pragma unroll
  for (int k = 0; k < 3; ++k) {
    int i = t + k * 1024;
    bk[k] = (int)(((u32)dv[k] * M) >> 24);
    rk[k] = 0;
    if (i < n) rk[k] = atomicAdd(&cnt[w * NBK + bk[k]], 1);
  }
  bar();
  int ex = 0;
  if (t < NBK) {
    u32 cv[16]; int tot = 0;
#pragma unroll
    for (int k = 0; k < 16; ++k) cv[k] = cnt[k * NBK + t];
#pragma unroll
    for (int k = 0; k < 16; ++k) { u32 v = cv[k]; cv[k] = tot; tot += v; }
    int inc = wave_incl_scan(tot, lane);
    ex = inc - tot;
#pragma unroll
    for (int k = 0; k < 16; ++k) cnt[k * NBK + t] = cv[k] + ex;
    if (lane == 63) wt[w] = inc;
  }
  bar();
  const int w0 = wt[0], w1 = w0 + wt[1], w2 = w1 + wt[2];
  if (t < NBK) tab[c * TABS + t] = (u16)(ex + (w == 0 ? 0 : w == 1 ? w0 : w == 2 ? w1 : w2));
  if (t == NBK) tab[c * TABS + NBK] = (u16)n;
#pragma unroll
  for (int k = 0; k < 3; ++k) {
    int i = t + k * 1024;
    int hb = bk[k] >> 6;
    int p = cnt[w * NBK + bk[k]] + rk[k] + (hb == 0 ? 0 : hb == 1 ? w0 : hb == 2 ? w1 : w2);
    if (i < n) {
      sM[p * 3 + 0] = y[k][0] | (y[k][1] << 16);
      sM[p * 3 + 1] = y[k][2] | ((u32)dv[k] << 16);
      sM[p * 3 + 2] = sv[k];
    }
#pragma unroll
    for (int j = 0; j < 2; ++j) {
      int pp = shfl(p, j * 32 + (lane >> 1));
      int q = (w * 64 + k * 1024) * 2 + j * 64 + lane;
      if (q < 2 * n) sA[pp * 2 + (lane & 1)] = av[k][j];
    }
  }
  bar();
  u32x4* oa = recA + (size_t)c * CHCAP * 2;
#pragma unroll
  for (int k = 0; k < 5; ++k) { int i = t + k * 1024; if (i < 2 * n) oa[i] = sA[i]; }
  u32x4* om = (u32x4*)(recM + (size_t)c * CHCAP * 3);
#pragma unroll
  for (int k = 0; k < 2; ++k) { int i = t + k * 1024; if (i * 4 < 3 * n) om[i] = ((u32x4*)sM)[i]; }
}

struct Acc { f32x16 v[2][2][2]; };

__device__ __forceinline__ float dot16(const f32x16& a, u32 waddr, float init) {
  float s = init;
#pragma unroll
  for (int q = 0; q < 4; ++q) {
    f32x4 wv = *LDSP(const f32x4, waddr + q * 512);
#pragma unroll
    for (int i = 0; i < 4; ++i) s = fmaf(a[q * 4 + i], wv[i], s);
  }
  return s;
}
__device__ __forceinline__ void dot16x2(const f32x16& a, const f32x16& b, u32 waddr, float& sa, float& sb) {
#pragma unroll
  for (int q = 0; q < 4; ++q) {
    f32x4 wv = *LDSP(const f32x4, waddr + q * 512);
#pragma unroll
    for (int i = 0; i < 4; ++i) { sa = fmaf(a[q * 4 + i], wv[i], sa); sb = fmaf(b[q * 4 + i], wv[i], sb); }
  }
}

__global__ __launch_bounds__(512, 2) NOPK void k_main(const u16* __restrict__ x, const u16* __restrict__ wrad, const u32x4* __restrict__ recA,
                                                 const u32* __restrict__ recM, const u16* __restrict__ tab, const u32x4* __restrict__ wfrag,
                                                 const float* __restrict__ epsf, u16* __restrict__ out, int N, u32 M) {
  __shared__ __attribute__((aligned(16))) char smem[L_TOTAL];
  const u32 LB = (u32)(uintptr_t)(__attribute__((address_space(3))) char*)smem;
  const int t = threadIdx.x, lane = t & 63;
  const int w = __builtin_amdgcn_readfirstlane(t >> 6);
  const int b = (blockIdx.x & 7) * 32 + (blockIdx.x >> 3);
  const int n0 = (int)min((u32)N, (((u32)b << 24) + M - 1) / M);
  const int n1 = b == NBK - 1 ? N : (int)min((u32)N, (((u32)(b + 1) << 24) + M - 1) / M);
  const int nn = n1 - n0;

  int st = 0, cn = 0;
  if (t < NBK) { st = tab[t * TABS + b]; cn = tab[t * TABS + b + 1] - st; }
  u32 wr[3][4];
#pragma unroll
  for (int k = 0; k < 3; ++k) {
    int task = min(t + k * 512, 1279);
    int q = task / 320, pc = task - q * 320;
#pragma unroll
    for (int i = 0; i < 4; ++i) wr[k][i] = wrad[(q * 4 + i) * 320 + pc];
  }
  {
    const int nclr = (L_TOTAL - L_CNT) / 16;
    for (int i = t; i < nclr; i += 512) *LDSP(u32x4, LB + L_CNT + i * 16) = u32x4{0, 0, 0, 0};
  }
  bar();
  int inc = wave_incl_scan(cn, lane);
  if (t < NBK && lane == 63) *LDSP(int, LB + L_WT + w * 4) = inc;
  if (t >= 256 && t < 264) *LDSP(float, LB + L_ONE + (t - 256) * 4) = 1.0f;
  bar();
  const u32x4 wt = *LDSP(const u32x4, LB + L_WT);
  const int total = __builtin_amdgcn_readfirstlane(wt[0] + wt[1] + wt[2] + wt[3]);
  bool fast = total <= RCAP;
  if (fast && t < NBK) {
    int o = inc - cn + (w > 0 ? wt[0] : 0) + (w > 1 ? wt[1] : 0) + (w > 2 ? wt[2] : 0);
    for (int i = 0; i < cn; ++i) *LDSP(int, LB + L_W + (o + i) * 4) = t * CHCAP + st + i;
  }
  bar();
  u32x3 mv[6]; u32x4 av[6][2]; int rk[6];
  if (fast) {
#pragma unroll
    for (int k = 0; k < 6; ++k) {
      int i = k * 512 + t;
      int ri = *LDSP(const int, LB + L_W + (i < total ? i : 0) * 4);
      if (total == 0) ri = 0;
      mv[k] = *(const u32x3*)(recM + (size_t)ri * 3);
    }
#pragma unroll
    for (int k = 0; k < 6; ++k) {
#pragma unroll
      for (int j = 0; j < 2; ++j) {
        int i = k * 512 + w * 64 + j * 32 + (lane >> 1);
        int ri = *LDSP(const int, LB + L_W + (i < total ? i : 0) * 4);
        if (total == 0) ri = 0;
        av[k][j] = recA[(size_t)ri * 2 + (lane & 1)];
      }
    }
#pragma unroll
    for (int k = 0; k < 6; ++k) {
      int i = k * 512 + t;
      int ln = (int)(mv[k][1] >> 16) - n0;
      rk[k] = 0;
      if (i < total) rk[k] = __hip_atomic_fetch_add(LDSP(int, LB + L_CNT + ln * 32 + w * 4), 1, __ATOMIC_RELAXED, __HIP_MEMORY_SCOPE_WORKGROUP);
    }
  }
  bar();
#pragma unroll
  for (int k = 0; k < 3; ++k) {
    int task = t + k * 512;
    int q = task / 320, pc = task - q * 320, p = pc >> 6, c = pc & 63;
    if (task < 1280) *LDSP(u32x4, LB + L_W + p * 4096 + (c & 1) * 2048 + q * 512 + (c >> 1) * 16) = u32x4{wr[k][0] << 16, wr[k][1] << 16, wr[k][2] << 16, wr[k][3] << 16};
  }
  int ncnt, npre, narea;
  {
    u32x4 c0 = *LDSP(const u32x4, LB + L_CNT + lane * 32), c1 = *LDSP(const u32x4, LB + L_CNT + lane * 32 + 16);
    int cc[8] = {(int)c0[0], (int)c0[1], (int)c0[2], (int)c0[3], (int)c1[0], (int)c1[1], (int)c1[2], (int)c1[3]};
    ncnt = 0; npre = 0;
#pragma unroll
    for (int k = 0; k < 8; ++k) { if (k == w) npre = ncnt; ncnt += cc[k]; }
    int ng = lane < nn ? max(MINGRP, (ncnt + 7) >> 3) : 0;
    int gi = wave_incl_scan(ng, lane);
    int gtot = __builtin_amdgcn_readlane(gi, 63);
    fast = fast && gtot <= GCAP;
    narea = fast ? L_REG + (gi - ng) * GRP : L_REG + lane * (MINGRP * GRP);
    if (lane >= nn) narea = L_REG;
    if (w == 0) *LDSP(int, LB + L_NI + lane * 4) = narea;
  }
  const int r = lane & 31, h = lane >> 5;
  if (fast) {
#pragma unroll
    for (int k = 0; k < 6; ++k) {
      int i = k * 512 + t;
      int ln = ((int)(mv[k][1] >> 16) - n0) & 63;
      int slot = shfl(npre, ln) + rk[k];
      int area = shfl(narea, ln);
      int ga = area + (slot >> 3) * GRP, sj = slot & 7;
      if (i < total) {
        *LDSP(u32, LB + ga + 256 + sj * 4) = mv[k][0] << 16;
        *LDSP(u32, LB + ga + 288 + sj * 4) = mv[k][0] & 0xffff0000u;
        *LDSP(u32, LB + ga + 320 + sj * 4) = mv[k][1] << 16;
        int sp = slot < 16 ? (slot & 3) | ((slot & 4) << 1) | ((slot & 8) >> 1) : slot;
        *LDSP(u16, LB + area + (sp >> 3) * GRP + 352 + (sp & 7) * 2) = (u16)(N - (int)mv[k][2]);
      }
      int aa = ga + sj * 32;
#pragma unroll
      for (int j = 0; j < 2; ++j) {
        int a2 = shfl(aa, j * 32 + (lane >> 1));
        int i2 = k * 512 + w * 64 + j * 32 + (lane >> 1);
        if (i2 < total) *LDSP(u32x4, LB + a2 + (lane & 1) * 16) = av[k][j];
      }
    }
  }
  bar();

  if (fast) {
    const __amdgpu_buffer_rsrc_t xr = __builtin_amdgcn_make_buffer_rsrc((void*)x, (short)0, N * 512, 0x00020000);
    const int ninfo = narea | (max(MINGRP, (ncnt + 7) >> 3) << 18);
    const int nblk = max(1, (ncnt + 15) >> 4);
    const int d = (r & 3) + 4 * (r >> 3), sl = (r >> 2) & 1;
    const u32 c_a = LB + d * 2, c_y1 = 256 + (1 + sl) * 32 - d * 2, c_y0 = sl ? 256 - d * 2 : LB + L_ONE;
    const u32 c_s = LB + 352 + 8 * h;
    const int c_x = N * 512 + r * 4;
    const u32 wc = LB + L_W + r * 16;
    const u32 wA = wc + (h ? 2 : 0) * 4096, wC = wc + (h ? 4 : 3) * 4096;
    const int idxe = 0x88888888, idxo = 0xdddddddd;
    const int m512 = -512;

    int nsteps = 0;
    auto build = [&](int lbase) AINL {
      int v = L_ZG, s0 = 0;
      const int si = lbase + lane;
      for (int pn = w; pn < nn; pn += 8) {
        int inf = __builtin_amdgcn_readlane(ninfo, pn), nb = __builtin_amdgcn_readlane(nblk, pn);
        int o = si - s0, hf = o >= nb ? 1 : 0, bi = o - hf * nb;
        int dsc = ((inf & 0x3ffff) + bi * (2 * GRP)) | (2 * bi + 1 < (inf >> 18) ? 1 << 18 : 0) | (bi == 0 ? 1 << 19 : 1 << 20) | (bi == nb - 1 ? 1 << (21 + hf) : 0) | (hf << 23);
        if (o >= 0 && o < 2 * nb) v = dsc;
        s0 += 2 * nb;
      }
      nsteps = s0;
      return v;
    };
    int list = build(0), pi = 0;
    auto next = [&]() AINL {
      int v = __builtin_amdgcn_readlane(list, pi & 63);
      ++pi;
      if ((pi & 63) == 0) list = build(pi);
      return v;
    };
    auto issue = [&](int ds, u32x8 (&rb)[2]) AINL {
      const int g0 = ds & 0x3ffff, g1 = (ds & (1 << 18)) ? g0 + GRP : L_ZG;
      u32x2 s0 = *LDSP(const u32x2, c_s + g0), s1 = *LDSP(const u32x2, c_s + g1);
      u32 sv[4] = {s0[0], s0[1], s1[0], s1[1]};
      int so = (ds >> 15) & 256;
#pragma unroll
      for (int i = 0; i < 8; ++i) {
        int off;
        if (i & 1) asm("v_mad_i32_i16 %0, %1, %2, %3 op_sel:[1,0,0,0]" : "=v"(off) : "v"(sv[i >> 1]), "s"(m512), "v"(c_x));
        else asm("v_mad_i32_i16 %0, %1, %2, %3" : "=v"(off) : "v"(sv[i >> 1]), "s"(m512), "v"(c_x));
        rb[0][i] = __builtin_amdgcn_raw_buffer_load_b32(xr, off, so, 0);
        rb[1][i] = __builtin_amdgcn_raw_buffer_load_b32(xr, off + 128, so, 0);
      }
    };
    Acc acc;
    u32 hold[4]; float dg[2];
    int uarea = 0;
    auto step = [&](int ds, const u32x8 (&rb)[2]) AINL {
      const int g0 = ds & 0x3ffff, dg1 = (ds & (1 << 18)) ? GRP : L_ZG - g0;
      u32 ga = c_a + g0 + h * dg1;
      u32 y0a = sl ? ga + c_y0 : c_y0, y1a = ga + c_y1;
      float a[8];
#pragma unroll
      for (int j = 0; j < 8; ++j) a[j] = bf2f(*LDSP(const u16, ga + j * 32));
      f32x4 ya = *LDSP(const f32x4, y0a), yb = *LDSP(const f32x4, y0a + 16), yc = *LDSP(const f32x4, y1a), yd = *LDSP(const f32x4, y1a + 16);
      u32x4 G[2];
      G[0] = u32x4{cvtpk(a[0] * ya[0], a[1] * ya[1]), cvtpk(a[2] * ya[2], a[3] * ya[3]), cvtpk(a[4] * yb[0], a[5] * yb[1]), cvtpk(a[6] * yb[2], a[7] * yb[3])};
      G[1] = u32x4{cvtpk(a[0] * yc[0], a[1] * yc[1]), cvtpk(a[2] * yc[2], a[3] * yc[3]), cvtpk(a[4] * yd[0], a[5] * yd[1]), cvtpk(a[6] * yd[2], a[7] * yd[3])};
      asm volatile("" :: "v"(rb[0]), "v"(rb[1]));
      if (ds & (1 << 19)) {
        const f32x16 z = {0, 0, 0, 0, 0, 0, 0, 0, 0, 0, 0, 0, 0, 0, 0, 0};
#pragma unroll
        for (int g = 0; g < 2; ++g) {
          u32x4 be, bo;
#pragma unroll
          for (int i = 0; i < 4; ++i) {
            be[i] = __builtin_amdgcn_perm(rb[g][2 * i + 1], rb[g][2 * i], 0x05040100);
            bo[i] = __builtin_amdgcn_perm(rb[g][2 * i + 1], rb[g][2 * i], 0x07060302);
          }
#pragma unroll
          for (int m = 0; m < 2; ++m) {
            acc.v[m][g][0] = __builtin_amdgcn_mfma_f32_32x32x16_bf16(__builtin_bit_cast(bf16x8, G[m]), __builtin_bit_cast(bf16x8, be), z, 0, 0, 0);
            acc.v[m][g][1] = __builtin_amdgcn_mfma_f32_32x32x16_bf16(__builtin_bit_cast(bf16x8, G[m]), __builtin_bit_cast(bf16x8, bo), z, 0, 0, 0);
          }
        }
        uarea = g0;
      }
      if (ds & (1 << 20)) {
#pragma unroll
        for (int g = 0; g < 2; ++g)
#pragma unroll
          for (int m = 0; m < 2; ++m) {
            acc.v[m][g][0] = __builtin_amdgcn_smfmac_f32_32x32x32_bf16(__builtin_bit_cast(bf16x8, G[m]), __builtin_bit_cast(bf16x16, rb[g]), acc.v[m][g][0], idxe, 0, 0);
            acc.v[m][g][1] = __builtin_amdgcn_smfmac_f32_32x32x32_bf16(__builtin_bit_cast(bf16x8, G[m]), __builtin_bit_cast(bf16x16, rb[g]), acc.v[m][g][1], idxo, 0, 0);
          }
      }
      {
        if (ds & (1 << 21)) {
          float u[4][2];
#pragma unroll
          for (int p = 0; p < 2; ++p) {
            u[0][p] = dot16(acc.v[0][0][p], wA + p * 2048, 0.f);
            u[1][p] = dot16(acc.v[1][0][p], wc + 2 * 4096 + p * 2048, 0.f);
            u[2][p] = dot16(acc.v[0][1][p], wC + p * 2048, 0.f);
            u[3][p] = dot16(acc.v[1][1][p], wc + 4 * 4096 + p * 2048, 0.f);
            dg[p] = dot16(acc.v[0][1][p], wc + 1 * 4096 + p * 2048, 0.f);
          }
#pragma unroll
          for (int i = 0; i < 4; ++i) hold[i] = cvtpk(u[i][0], u[i][1]);
        }
        if (ds & (1 << 22)) {
          float u[4][2], d2[2];
#pragma unroll
          for (int p = 0; p < 2; ++p) {
            u[0][p] = u[1][p] = u[2][p] = u[3][p] = d2[p] = 0.f;
            dot16x2(acc.v[0][0][p], acc.v[0][1][p], wC + p * 2048, u[0][p], u[2][p]);
            dot16x2(acc.v[1][0][p], acc.v[1][1][p], wc + 4 * 4096 + p * 2048, u[1][p], u[3][p]);
            dot16x2(acc.v[1][0][p], acc.v[1][1][p], wc + 1 * 4096 + p * 2048, d2[p], dg[p]);
          }
          u32 ua = LB + uarea + h * 512 + r * 4;
          *LDSP(u32, ua + 0 * 128) = hold[0];
          *LDSP(u32, ua + 8 * 128) = hold[1];
          *LDSP(u32, ua + 1 * 128) = hold[2];
          *LDSP(u32, ua + 9 * 128) = hold[3];
          *LDSP(u32, ua + 2 * 128) = cvtpk(u[0][0], u[0][1]);
          *LDSP(u32, ua + 10 * 128) = cvtpk(u[1][0], u[1][1]);
          *LDSP(u32, ua + 3 * 128) = cvtpk(u[2][0], u[2][1]);
          *LDSP(u32, ua + 11 * 128) = cvtpk(u[3][0], u[3][1]);
          *LDSP(u32, ua - h * 384 + 16 * 128) = cvtpk(h ? dg[0] : d2[0], h ? dg[1] : d2[1]);
        }
      }
    };
    u32x8 r0[2], r1[2], r2[2];
    int d0 = next(); issue(d0, r0);
    __builtin_amdgcn_sched_barrier(0);
    int d1 = next(); issue(d1, r1);
    __builtin_amdgcn_sched_barrier(0);
    for (int it = 0; it < nsteps; it += 3) {
      int d2 = next(); issue(d2, r2); step(d0, r0);
      d0 = next(); issue(d0, r0); step(d1, r1);
      d1 = next(); issue(d1, r1); step(d2, r2);
    }
  } else {
    for (int ln = w; ln < nn; ln += 8) {
      float U[16], tr = 0.f;
#pragma unroll
      for (int i = 0; i < 16; ++i) U[i] = 0.f;
      for (int c = 0; c < NBK; ++c) {
        int s0 = tab[c * TABS + b], s1 = tab[c * TABS + b + 1];
        for (int q = s0; q < s1; ++q) {
          size_t ri = (size_t)c * CHCAP + q;
          u32 m0 = recM[ri * 3], m1 = recM[ri * 3 + 1], m2 = recM[ri * 3 + 2];
          if ((int)(m1 >> 16) != n0 + ln) continue;
          const u16* ap = (const u16*)(recA + ri * 2);
          float R[5] = {0.f, 0.f, 0.f, 0.f, 0.f};
          for (int dd = 0; dd < 16; ++dd) {
            float a = bf2f(ap[dd]);
#pragma unroll
            for (int p = 0; p < 5; ++p) R[p] = fmaf(a, *LDSP(const float, LB + L_W + p * 4096 + (lane & 1) * 2048 + (dd >> 2) * 512 + (lane >> 1) * 16 + (dd & 3) * 4), R[p]);
          }
          float Y[4] = {1.f, bf2f(m0 & 0xffff), bf2f(m0 >> 16), bf2f(m1 & 0xffff)};
#pragma unroll
          for (int tt = 0; tt < 4; ++tt) {
            float f = bf2f(x[(size_t)m2 * 256 + tt * 64 + lane]);
#pragma unroll
            for (int s = 0; s < 4; ++s) U[s * 4 + tt] += Y[s] * f * R[tt == 0 ? (s == 0 ? 0 : 2) : (s == 0 ? 3 : 4)];
            if (tt > 0) tr += Y[tt] * f * R[1];
          }
        }
      }
      u32 ua = LB + L_REG + ln * (MINGRP * GRP) + lane * 2;
#pragma unroll
      for (int i = 0; i < 16; ++i) *LDSP(u16, ua + i * 128) = (u16)cvtpk(U[i], 0.f);
      *LDSP(u16, ua + 16 * 128) = (u16)cvtpk(tr, 0.f);
      *LDSP(u16, ua + 17 * 128) = 0;
    }
  }
  const int tile = w >> 1, uu = w & 1;
  u32x4 wf[5][2][2];
  if (tile * 16 < nn) {
#pragma unroll
    for (int blk = 0; blk < 5; ++blk)
#pragma unroll
      for (int ks = 0; ks < 2; ++ks)
#pragma unroll
        for (int nt = 0; nt < 2; ++nt) wf[blk][ks][nt] = wfrag[(((blk * 2 + ks) * 2 + uu) * 2 + nt) * 64 + lane];
  }
  bar();
  if (tile * 16 < nn) {
    const int node = tile * 16 + (lane & 15), q = lane >> 4;
    const u32 ua = LB + *LDSP(const int, LB + L_NI + node * 4) + q * 16;
    auto mm = [&](int blk, int vec, f32x4 (&ac)[2]) AINL {
#pragma unroll
      for (int ks = 0; ks < 2; ++ks) {
        u32x4 bv = *LDSP(const u32x4, ua + vec * 128 + ks * 64);
#pragma unroll
        for (int nt = 0; nt < 2; ++nt)
          ac[nt] = __builtin_amdgcn_mfma_f32_16x16x32_bf16(__builtin_bit_cast(bf16x8, wf[blk][ks][nt]), __builtin_bit_cast(bf16x8, bv), ac[nt], 0, 0, 0);
      }
    };
    const f32x4 z4 = {0, 0, 0, 0};
    f32x4 a0[2] = {z4, z4}, a1[3][2] = {{z4, z4}, {z4, z4}, {z4, z4}};
    mm(0, 0, a0); mm(1, 16, a0); mm(1, 17, a0);
#pragma unroll
    for (int i = 0; i < 3; ++i) { mm(2, (i + 1) * 4, a1[i]); mm(3, i + 1, a1[i]); }
#pragma unroll
    for (int j = 0; j < 3; ++j)
#pragma unroll
      for (int k = 0; k < 3; ++k) {
        f32x4 v[2] = {z4, z4};
        mm(4, (j + 1) * 4 + k + 1, v);
#pragma unroll
        for (int i = 0; i < 3; ++i) {
          float e = epsf[i * 9 + j * 3 + k];
          a1[i][0] += e * v[0]; a1[i][1] += e * v[1];
        }
      }
    if (node < nn) {
      u16* op = out + (size_t)(n0 + node) * 256 + uu * 32 + q * 8;
      float g[8];
#pragma unroll
      for (int i = 0; i < 8; ++i) g[i] = 1.f / (1.f + __expf(-a0[i >> 2][i & 3]));
      *(u32x4*)op = u32x4{cvtpk(a0[0][0], a0[0][1]), cvtpk(a0[0][2], a0[0][3]), cvtpk(a0[1][0], a0[1][1]), cvtpk(a0[1][2], a0[1][3])};
#pragma unroll
      for (int i = 0; i < 3; ++i)
        *(u32x4*)(op + 64 + i * 64) = u32x4{cvtpk(a1[i][0][0] * g[0], a1[i][0][1] * g[1]), cvtpk(a1[i][0][2] * g[2], a1[i][0][3] * g[3]),
                                            cvtpk(a1[i][1][0] * g[4], a1[i][1][1] * g[5]), cvtpk(a1[i][1][2] * g[6], a1[i][1][3] * g[7])};
    }
  }
}

extern "C" void kernel_launch(void* const* d_in, const int* in_sizes, int n_in, void* d_out, int out_size, void* d_ws, size_t ws_size,
                              hipStream_t stream) {
  const u16* x = (const u16*)d_in[0]; const u16* rhat = (const u16*)d_in[1]; const u32x4* ea = (const u32x4*)d_in[2];
  const int* src = (const int*)d_in[3]; const int* dst = (const int*)d_in[4];
  const u16* wrad = (const u16*)d_in[5]; const u16* wsi0 = (const u16*)d_in[6]; const u16* wsi1 = (const u16*)d_in[7]; const u16* eps = (const u16*)d_in[8];
  const int N = in_sizes[0] / 256, E = in_sizes[3];
  const int chunk = (E + NBK - 1) / NBK;
  const u32 M = (u32)(((unsigned long long)NBK << 24) / (unsigned long long)N);
  char* ws = (char*)d_ws;
  u32x4* recA = (u32x4*)ws;
  u32* recM = (u32*)(ws + (size_t)NBK * CHCAP * 32);
  u16* tab = (u16*)(ws + (size_t)NBK * CHCAP * 44);
  u16* wfrag = tab + NBK * TABS;
  float* epsf = (float*)(wfrag + 20480);
  k_bin<<<NBK, 1024, 0, stream>>>(rhat, ea, src, dst, wsi0, wsi1, eps, recA, recM, tab, wfrag, epsf, E, chunk, M);
  k_main<<<NBK, 512, 0, stream>>>(x, wrad, recA, recM, tab, (const u32x4*)wfrag, epsf, (u16*)d_out, N, M);
}
```

```cpp
#include <hip/hip_runtime.h>
#include <stdint.h>

typedef unsigned short u16;
typedef unsigned u32;
typedef __attribute__((ext_vector_type(2))) u32 u32x2;
typedef __attribute__((ext_vector_type(3))) u32 u32x3;
typedef __attribute__((ext_vector_type(4))) u32 u32x4;
typedef __attribute__((ext_vector_type(8))) u32 u32x8;
typedef __attribute__((ext_vector_type(4))) float f32x4;
typedef __attribute__((ext_vector_type(16))) float f32x16;
typedef __attribute__((ext_vector_type(8))) __bf16 bf16x8;
typedef __attribute__((ext_vector_type(16))) __bf16 bf16x16;
#define LDSP(T, a) ((__attribute__((address_space(3))) T*)(uintptr_t)(u32)(a))

#define AINL __attribute__((always_inline))

#define TS(k, p)
#define TC(k, p)
constexpr int NBK = 256;
constexpr int NCH = 512;
constexpr int CHCAP = 1280;
constexpr int TABS = 264;
constexpr int MINBLK = 4;
constexpr int NSL = 7;

constexpr int L_NI = 0;
constexpr int L_W = 512;
constexpr int L_MAP = L_W + 10240;
constexpr int L_LIST = L_MAP + 8 * NSL * 256;
constexpr int L_CNT = L_LIST + 4096;
constexpr int L_KM = L_CNT + 2048;
constexpr int L_YS = L_KM + 272;
constexpr int L_TOTAL = 163840;
constexpr int BCAP = (L_TOTAL - L_YS) / 672;
constexpr int L_A = L_TOTAL - BCAP * 512;
static_assert(L_YS + (BCAP + 3) * 160 <= L_A && (BCAP + 3) * 64 <= 8 * NSL * 256 && BCAP <= 208 && L_A % 512 == 0, "");

__device__ __forceinline__ float bf2f(u32 v) { return __uint_as_float(v << 16); }
typedef __attribute__((ext_vector_type(2))) __bf16 bf16x2;
typedef __attribute__((ext_vector_type(2))) float f32x2;
__device__ __forceinline__ u32 cvtpk(float lo, float hi) { f32x2 v = {lo, hi}; return __builtin_bit_cast(u32, __builtin_convertvector(v, bf16x2)); }
__device__ __forceinline__ void bar() { asm volatile("s_waitcnt lgkmcnt(0)\n\ts_barrier" ::: "memory"); }
__device__ __forceinline__ int shfl(int v, int l) { return __builtin_amdgcn_ds_bpermute(l << 2, v); }
typedef __attribute__((ext_vector_type(2))) u16 u16x2;
typedef __attribute__((ext_vector_type(2))) _Float16 f16x2;
__device__ __forceinline__ u32 pkmax(u32 a, u32 b) { return __builtin_bit_cast(u32, __builtin_elementwise_max(__builtin_bit_cast(u16x2, a), __builtin_bit_cast(u16x2, b))); }
__device__ __forceinline__ u32 maxexp8(u32x4 v) {
  u32 m = pkmax(pkmax(v[0] & 0x7fff7fffu, v[1] & 0x7fff7fffu), pkmax(v[2] & 0x7fff7fffu, v[3] & 0x7fff7fffu));
  return max(m >> 16, m & 0xffffu) >> 7;
}
template <int CTRL> __device__ __forceinline__ u32 dppmax(u32 v) { return max(v, (u32)__builtin_amdgcn_update_dpp(0, (int)v, CTRL, 0xf, 0xf, true)); }
__device__ __forceinline__ u32 tof16(u32 v, float sc) {
  f16x2 r = {(_Float16)(__uint_as_float(v << 16) * sc), (_Float16)(__uint_as_float(v & 0xffff0000u) * sc)};
  return __builtin_bit_cast(u32, r);
}
__device__ __forceinline__ u32x4 tof16x8(u32x4 v, u32 e) { float sc = __uint_as_float(e << 23); return u32x4{tof16(v[0], sc), tof16(v[1], sc), tof16(v[2], sc), tof16(v[3], sc)}; }
template <int CTRL, int RM> __device__ __forceinline__ int dppadd(int v) { return v + __builtin_amdgcn_update_dpp(0, v, CTRL, RM, 0xf, false); }
__device__ __forceinline__ int wave_incl_scan(int v) {
  v = dppadd<0x111, 0xf>(v); v = dppadd<0x112, 0xf>(v); v = dppadd<0x114, 0xf>(v); v = dppadd<0x118, 0xf>(v);
  v = dppadd<0x142, 0xa>(v); v = dppadd<0x143, 0xc>(v);
  return v;
}
#define NTL 1
#define LDG(p) __builtin_nontemporal_load(p)
#define STP 0
#define STO 16
#define STX 0
template <int POL> __device__ __forceinline__ void store_wt(void* base, u32 off, u32x4 v) {
  __builtin_amdgcn_raw_buffer_store_b128(v, __builtin_amdgcn_make_buffer_rsrc(base, 0, 0x7fffffff, 0x20000), off, 0, POL);
}

__global__ __launch_bounds__(512, 4) void k_bin(const u16* __restrict__ rhat, const int* __restrict__ src,
                                                const int* __restrict__ dst, const u16* __restrict__ wsi0, const u16* __restrict__ wsi1,
                                                const u16* __restrict__ eps, u32x4* __restrict__ recA, u32* __restrict__ recM,
                                                u16* __restrict__ tab, u16* __restrict__ wfrag, float* __restrict__ epsf,
                                                const u32x4* __restrict__ x, const u32x4* __restrict__ wrad, u32x4* __restrict__ xh,
                                                unsigned char* __restrict__ exb, u32* __restrict__ wimg, int E, int chunk, u32 M, int N, int rpb) {
  __shared__ __attribute__((aligned(16))) u32 sM[CHCAP * 3];
  __shared__ __attribute__((aligned(16))) u32 cnt[8 * NBK];
  __shared__ u32 wt[4];
  const int t = threadIdx.x, lane = t & 63, c = (int)blockIdx.x - 6;
  const int w = __builtin_amdgcn_readfirstlane(t >> 6);
  if (c < 0) {
    if (c < -1) {
      const int blk = blockIdx.x, nt = (t >> 6) & 1, u = (t >> 7) & 1, ks = t >> 8, rho = lane & 15, q = lane >> 4;
      const u16* p = (blk < 2 ? wsi0 + blk * 4096 : wsi1 + (blk - 2) * 4096) + (ks * 32 + q * 8) * 64 + 32 * u + 8 * (rho >> 2) + 4 * nt + (rho & 3);
      u32 f[8];
#pragma unroll
      for (int j = 0; j < 8; ++j) f[j] = p[j * 64];
      ((u32x4*)wfrag)[blk * 512 + t] = u32x4{f[0] | (f[1] << 16), f[2] | (f[3] << 16), f[4] | (f[5] << 16), f[6] | (f[7] << 16)};
      return;
    }
    if (w == 1) {
      float ev = bf2f(eps[min(lane, 26)]);
      bool dg = lane < 27 && (lane % 9) % 4 == 0 && ev != 0.f;
      unsigned long long any = __builtin_amdgcn_ballot_w64(dg);
      if (lane < 27) epsf[lane] = ev;
      if (lane == 29) epsf[29] = __int_as_float(any ? 1 : 0);
    }
    if (w == 0) {
      u32x4 wv[10]; u32 m = 16;
#pragma unroll
      for (int k = 0; k < 10; ++k) wv[k] = wrad[k * 64 + lane];
#pragma unroll
      for (int k = 0; k < 10; ++k) m = max(m, maxexp8(wv[k]));
      m = dppmax<0x140>(dppmax<0x141>(dppmax<0x4e>(dppmax<0xb1>(m))));
      m = max(max((u32)__builtin_amdgcn_readlane(m, 0), (u32)__builtin_amdgcn_readlane(m, 16)), max((u32)__builtin_amdgcn_readlane(m, 32), (u32)__builtin_amdgcn_readlane(m, 48)));
#pragma unroll
      for (int h = 0; h < 10; ++h) {
        int i = h * 64 + lane, dd = i / 40, cp = (i - dd * 40) * 4;
        u32x4 o = tof16x8(wv[h], 268 - m);
#pragma unroll
        for (int k = 0; k < 4; ++k) wimg[((cp + k) >> 5) * 512 + (dd >> 2) * 128 + ((cp + k) & 31) * 4 + (dd & 3)] = o[k];
      }
      if (lane == 0) epsf[28] = __int_as_float((int)m);
    }
    return;
  }
  TS(0, 0);
  const int e0 = min(c * chunk, E - 1);
  const int n = max(0, min(chunk, E - c * chunk));
  int dv[3], sv[3]; u32 y[3][3];
#pragma unroll
  for (int k = 0; k < 3; ++k) {
    int i = t + k * 512; int e = e0 + (i < n ? i : 0);
    dv[k] = LDG(dst + e);
  }
  u32x4 xv[2];
  const int xr0 = c * rpb, xn = min(rpb, N - xr0);
#pragma unroll
  for (int k = 0; k < 2; ++k) {
    int i = (t >> 5) + k * 16;
    xv[k] = LDG(x + (size_t)(i < xn ? xr0 + i : 0) * 32 + (t & 31));
  }
#pragma unroll
  for (int k = 0; k < 3; ++k) {
    int i = t + k * 512; int e = e0 + (i < n ? i : 0);
    sv[k] = LDG(src + e);
#pragma unroll
    for (int j = 0; j < 3; ++j) y[k][j] = LDG(rhat + (size_t)e * 3 + j);
  }
  ((u32x4*)cnt)[t] = u32x4{0, 0, 0, 0};
  int bk[3], rk[3];
#pragma unroll
  for (int k = 0; k < 3; ++k) {
    int i = t + k * 512;
    bk[k] = (int)(((u32)dv[k] * M) >> 24);
    rk[k] = 0;
    if (i < n) rk[k] = atomicAdd(&cnt[w * NBK + bk[k]], 1);
  }
  bar();
  TS(0, 1);
  int ex = 0;
  if (t < NBK) {
    u32 cv[8]; int tot = 0;
#pragma unroll
    for (int k = 0; k < 8; ++k) cv[k] = cnt[k * NBK + t];
#pragma unroll
    for (int k = 0; k < 8; ++k) { u32 v = cv[k]; cv[k] = tot; tot += v; }
    int inc = wave_incl_scan(tot);
    ex = inc - tot;
#pragma unroll
    for (int k = 0; k < 8; ++k) cnt[k * NBK + t] = cv[k] + ex;
    if (lane == 63) wt[w] = inc;
  }
  bar();
  const int w0 = wt[0], w1 = w0 + wt[1], w2 = w1 + wt[2];
  u32 xm[2];
#pragma unroll
  for (int k = 0; k < 2; ++k) {
    int i = (t >> 5) + k * 16;
    u32 m = dppmax<0x140>(dppmax<0x141>(dppmax<0x4e>(dppmax<0xb1>(maxexp8(xv[k])))));
    m = max(max(m, (u32)__builtin_amdgcn_ds_swizzle(m, 0x401f)), 16u);
    store_wt<STX>(xh, i < xn ? (u32)(xr0 + i + (t & 16 ? N : 0)) * 256 + (t & 15) * 16 : 0x80000000u, tof16x8(xv[k], 268 - m));
    xm[k] = m;
  }
#pragma unroll
  for (int k = 0; k < 3; ++k) {
    if (k == 2 && 1024 + w * 64 >= n) break;
    int i = t + k * 512;
    int hb = bk[k] >> 6;
    int p = cnt[w * NBK + bk[k]] + rk[k] + (hb == 0 ? 0 : hb == 1 ? w0 : hb == 2 ? w1 : w2);
    if (i < n) {
      sM[p * 3 + 0] = y[k][0] | (y[k][1] << 16);
      u32 e = e0 + i;
      sM[p * 3 + 1] = y[k][2] | ((dv[k] & 255) << 16) | (e >> 16 << 24);
      sM[p * 3 + 2] = sv[k] | (e << 16);
    }
  }
  bar();
  TS(0, 2);
  u32x4* om = (u32x4*)(recM + (size_t)c * CHCAP * 3);
#pragma unroll
  for (int k = 0; k < 2; ++k) { int i = t + k * 512; if (i * 4 < 3 * n) store_wt<STP>(om, i * 16, ((u32x4*)sM)[i]); }
  if (t <= NBK) tab[c * TABS + t] = (u16)(t == NBK ? n : ex + (w == 0 ? 0 : w == 1 ? w0 : w == 2 ? w1 : w2));
#pragma unroll
  for (int k = 0; k < 2; ++k) { int i = (t >> 5) + k * 16; if ((t & 31) == 0 && i < xn) exb[xr0 + i] = (unsigned char)xm[k]; }
  TS(0, 3);
}

#define MAIN_D 4
#define MAIN_ASM \
  "s_mov_b32 s84, 0x100\n" \
  "s_mov_b32 s73, 0\n" \
  "s_mov_b32 s88, 32\n" \
  "s_mov_b64 s[92:93], %[sp]\n" \
  "s_mov_b32 s90, m0\n" \
  "s_mov_b32 s80, 0\n" \
  "s_mov_b64 s[78:79], %[xq]\n" \
  "s_mov_b32 s89, %[lb]\n" \
  "v_mbcnt_lo_u32_b32 v241, -1, 0\n" \
  "v_mbcnt_hi_u32_b32 v241, -1, v241\n" \
  "v_lshl_add_u32 v241, v241, 2, s89\n" \
  "ds_read_b32 v241, v241\n" \
  "s_mov_b32 s69, 0\n" \
  "s_mov_b32 s72, 0\n" \
  "s_mov_b64 s[74:75], %[ra]\n" \
  "s_sub_u32 s74, s74, 32\n" \
  "s_subb_u32 s75, s75, 0\n" \
  "s_getpc_b64 s[70:71]\n" \
  "s_add_u32 s70, s70, 12\n" \
  "s_addc_u32 s71, s71, 0\n" \
  "s_branch Lasub_%=\n" \
  "s_bitcmp0_b32 s73, 0\n" \
  "s_cbranch_scc1 Lp0_%=\n" \
  "s_mov_b32 m0, s81\n" \
  "s_bitcmp0_b32 s73, 4\n" \
  "s_cselect_b32 exec_hi, -1, 0\n" \
  "global_load_lds_dwordx4 v224, s[74:75]\n" \
  "s_mov_b32 exec_hi, -1\n" \
  "s_add_u32 s81, s81, 0x400\n" \
  "s_lshr_b32 s73, s73, 1\n" \
  "s_mov_b32 s88, s80\n" \
  "v_mov_b32 v224, v225\n" \
  "v_mov_b32 v225, v226\n" \
  "v_mov_b32 v226, v227\n" \
  "s_bitcmp0_b32 s73, 0\n" \
  "s_cbranch_scc1 Lp0_%=\n" \
  "s_mov_b32 m0, s81\n" \
  "s_bitcmp0_b32 s73, 4\n" \
  "s_cselect_b32 exec_hi, -1, 0\n" \
  "global_load_lds_dwordx4 v224, s[74:75]\n" \
  "s_mov_b32 exec_hi, -1\n" \
  "s_add_u32 s81, s81, 0x400\n" \
  "s_lshr_b32 s73, s73, 1\n" \
  "s_mov_b32 s88, s80\n" \
  "v_mov_b32 v224, v225\n" \
  "v_mov_b32 v225, v226\n" \
  "v_mov_b32 v226, v227\n" \
  "s_bitcmp0_b32 s73, 0\n" \
  "s_cbranch_scc1 Lp0_%=\n" \
  "s_mov_b32 m0, s81\n" \
  "s_bitcmp0_b32 s73, 4\n" \
  "s_cselect_b32 exec_hi, -1, 0\n" \
  "global_load_lds_dwordx4 v224, s[74:75]\n" \
  "s_mov_b32 exec_hi, -1\n" \
  "s_add_u32 s81, s81, 0x400\n" \
  "s_lshr_b32 s73, s73, 1\n" \
  "s_mov_b32 s88, s80\n" \
  "v_mov_b32 v224, v225\n" \
  "v_mov_b32 v225, v226\n" \
  "v_mov_b32 v226, v227\n" \
  "s_bitcmp0_b32 s73, 0\n" \
  "s_cbranch_scc1 Lp0_%=\n" \
  "s_mov_b32 m0, s81\n" \
  "s_bitcmp0_b32 s73, 4\n" \
  "s_cselect_b32 exec_hi, -1, 0\n" \
  "global_load_lds_dwordx4 v224, s[74:75]\n" \
  "s_mov_b32 exec_hi, -1\n" \
  "s_add_u32 s81, s81, 0x400\n" \
  "s_lshr_b32 s73, s73, 1\n" \
  "s_mov_b32 s88, s80\n" \
  "v_mov_b32 v224, v225\n" \
  "v_mov_b32 v225, v226\n" \
  "v_mov_b32 v226, v227\n" \
  "Lp0_%=:\n" \
  "v_mov_b32 v228, 0\n" \
  "v_mov_b32 v229, 0\n" \
  "v_lshlrev_b32 v232, 2, %[c_x]\n" \
  "v_lshlrev_b32 v233, 2, %[c_x]\n" \
  "v_lshlrev_b32 v234, 2, %[c_x]\n" \
  "v_lshlrev_b32 v235, 2, %[c_x]\n" \
  "v_lshlrev_b32 v236, 2, %[c_x]\n" \
  "v_lshlrev_b32 v237, 2, %[c_x]\n" \
  "s_mov_b32 exec_lo, 0\n" \
  "v_add_u32 v233, 4096, v233\n" \
  "v_add_u32 v235, 2048, v235\n" \
  "v_add_u32 v236, 6144, v236\n" \
  "s_not_b64 exec, exec\n" \
  "v_add_u32 v234, 4096, v234\n" \
  "v_add_u32 v237, 6144, v237\n" \
  "s_mov_b32 exec_hi, -1\n" \
  "s_waitcnt lgkmcnt(0)\n" \
  "v_readlane_b32 s64, v241, s80\n" \
  "s_add_u32 s80, s80, 1\n" \
  "v_readlane_b32 s65, v241, s80\n" \
  "s_add_u32 s80, s80, 1\n" \
  "v_readlane_b32 s66, v241, s80\n" \
  "s_add_u32 s80, s80, 1\n" \
  "v_readlane_b32 s67, v241, s80\n" \
  "s_add_u32 s80, s80, 1\n" \
  "s_nop 1\n" \
  "v_mad_u32_u16 v240, s64, 20, %[c_s]\n" \
  "ds_read_b128 v[208:211], v240\n" \
  "s_bitcmp1_b32 s64, 16\n" \
  "s_cselect_b64 s[76:77], s[78:79], %[xp]\n" \
  "s_waitcnt lgkmcnt(0)\n" \
  "v_mad_u32_u16 v136, v208, s84, %[c_x]\n" \
  "v_mad_u32_u16 v137, v208, s84, %[c_x] op_sel:[1,0,0,0]\n" \
  "v_mad_u32_u16 v138, v209, s84, %[c_x]\n" \
  "v_mad_u32_u16 v139, v209, s84, %[c_x] op_sel:[1,0,0,0]\n" \
  "v_mad_u32_u16 v140, v210, s84, %[c_x]\n" \
  "v_mad_u32_u16 v141, v210, s84, %[c_x] op_sel:[1,0,0,0]\n" \
  "v_mad_u32_u16 v142, v211, s84, %[c_x]\n" \
  "v_mad_u32_u16 v143, v211, s84, %[c_x] op_sel:[1,0,0,0]\n" \
  "global_load_dword v128, v136, s[76:77]\n" \
  "global_load_dword v136, v136, s[76:77] offset:128\n" \
  "global_load_dword v129, v137, s[76:77]\n" \
  "global_load_dword v137, v137, s[76:77] offset:128\n" \
  "global_load_dword v130, v138, s[76:77]\n" \
  "global_load_dword v138, v138, s[76:77] offset:128\n" \
  "global_load_dword v131, v139, s[76:77]\n" \
  "global_load_dword v139, v139, s[76:77] offset:128\n" \
  "global_load_dword v132, v140, s[76:77]\n" \
  "global_load_dword v140, v140, s[76:77] offset:128\n" \
  "global_load_dword v133, v141, s[76:77]\n" \
  "global_load_dword v141, v141, s[76:77] offset:128\n" \
  "global_load_dword v134, v142, s[76:77]\n" \
  "global_load_dword v142, v142, s[76:77] offset:128\n" \
  "global_load_dword v135, v143, s[76:77]\n" \
  "global_load_dword v143, v143, s[76:77] offset:128\n" \
  "v_mad_u32_u16 v240, s65, 20, %[c_s]\n" \
  "ds_read_b128 v[212:215], v240\n" \
  "s_bitcmp1_b32 s65, 16\n" \
  "s_cselect_b64 s[76:77], s[78:79], %[xp]\n" \
  "s_waitcnt lgkmcnt(0)\n" \
  "v_mad_u32_u16 v152, v212, s84, %[c_x]\n" \
  "v_mad_u32_u16 v153, v212, s84, %[c_x] op_sel:[1,0,0,0]\n" \
  "v_mad_u32_u16 v154, v213, s84, %[c_x]\n" \
  "v_mad_u32_u16 v155, v213, s84, %[c_x] op_sel:[1,0,0,0]\n" \
  "v_mad_u32_u16 v156, v214, s84, %[c_x]\n" \
  "v_mad_u32_u16 v157, v214, s84, %[c_x] op_sel:[1,0,0,0]\n" \
  "v_mad_u32_u16 v158, v215, s84, %[c_x]\n" \
  "v_mad_u32_u16 v159, v215, s84, %[c_x] op_sel:[1,0,0,0]\n" \
  "global_load_dword v144, v152, s[76:77]\n" \
  "global_load_dword v152, v152, s[76:77] offset:128\n" \
  "global_load_dword v145, v153, s[76:77]\n" \
  "global_load_dword v153, v153, s[76:77] offset:128\n" \
  "global_load_dword v146, v154, s[76:77]\n" \
  "global_load_dword v154, v154, s[76:77] offset:128\n" \
  "global_load_dword v147, v155, s[76:77]\n" \
  "global_load_dword v155, v155, s[76:77] offset:128\n" \
  "global_load_dword v148, v156, s[76:77]\n" \
  "global_load_dword v156, v156, s[76:77] offset:128\n" \
  "global_load_dword v149, v157, s[76:77]\n" \
  "global_load_dword v157, v157, s[76:77] offset:128\n" \
  "global_load_dword v150, v158, s[76:77]\n" \
  "global_load_dword v158, v158, s[76:77] offset:128\n" \
  "global_load_dword v151, v159, s[76:77]\n" \
  "global_load_dword v159, v159, s[76:77] offset:128\n" \
  "v_mov_b64 v[0:1], 0\n" \
  "v_mov_b64 v[2:3], 0\n" \
  "v_mov_b64 v[4:5], 0\n" \
  "v_mov_b64 v[6:7], 0\n" \
  "v_mov_b64 v[8:9], 0\n" \
  "v_mov_b64 v[10:11], 0\n" \
  "v_mov_b64 v[12:13], 0\n" \
  "v_mov_b64 v[14:15], 0\n" \
  "v_mov_b64 v[16:17], 0\n" \
  "v_mov_b64 v[18:19], 0\n" \
  "v_mov_b64 v[20:21], 0\n" \
  "v_mov_b64 v[22:23], 0\n" \
  "v_mov_b64 v[24:25], 0\n" \
  "v_mov_b64 v[26:27], 0\n" \
  "v_mov_b64 v[28:29], 0\n" \
  "v_mov_b64 v[30:31], 0\n" \
  "v_mov_b64 v[32:33], 0\n" \
  "v_mov_b64 v[34:35], 0\n" \
  "v_mov_b64 v[36:37], 0\n" \
  "v_mov_b64 v[38:39], 0\n" \
  "v_mov_b64 v[40:41], 0\n" \
  "v_mov_b64 v[42:43], 0\n" \
  "v_mov_b64 v[44:45], 0\n" \
  "v_mov_b64 v[46:47], 0\n" \
  "v_mov_b64 v[48:49], 0\n" \
  "v_mov_b64 v[50:51], 0\n" \
  "v_mov_b64 v[52:53], 0\n" \
  "v_mov_b64 v[54:55], 0\n" \
  "v_mov_b64 v[56:57], 0\n" \
  "v_mov_b64 v[58:59], 0\n" \
  "v_mov_b64 v[60:61], 0\n" \
  "v_mov_b64 v[62:63], 0\n" \
  "s_waitcnt vmcnt(32)\n" \
  "s_getpc_b64 s[70:71]\n" \
  "s_add_u32 s70, s70, 12\n" \
  "s_addc_u32 s71, s71, 0\n" \
  "s_branch Lasub_%=\n" \
  "v_mad_u32_u16 v240, s66, 20, %[c_s]\n" \
  "ds_read_b128 v[208:211], v240\n" \
  "s_bitcmp1_b32 s66, 16\n" \
  "s_cselect_b64 s[76:77], s[78:79], %[xp]\n" \
  "s_waitcnt lgkmcnt(0)\n" \
  "v_mad_u32_u16 v168, v208, s84, %[c_x]\n" \
  "v_mad_u32_u16 v169, v208, s84, %[c_x] op_sel:[1,0,0,0]\n" \
  "v_mad_u32_u16 v170, v209, s84, %[c_x]\n" \
  "v_mad_u32_u16 v171, v209, s84, %[c_x] op_sel:[1,0,0,0]\n" \
  "v_mad_u32_u16 v172, v210, s84, %[c_x]\n" \
  "v_mad_u32_u16 v173, v210, s84, %[c_x] op_sel:[1,0,0,0]\n" \
  "v_mad_u32_u16 v174, v211, s84, %[c_x]\n" \
  "v_mad_u32_u16 v175, v211, s84, %[c_x] op_sel:[1,0,0,0]\n" \
  "global_load_dword v160, v168, s[76:77]\n" \
  "global_load_dword v168, v168, s[76:77] offset:128\n" \
  "global_load_dword v161, v169, s[76:77]\n" \
  "global_load_dword v169, v169, s[76:77] offset:128\n" \
  "global_load_dword v162, v170, s[76:77]\n" \
  "global_load_dword v170, v170, s[76:77] offset:128\n" \
  "global_load_dword v163, v171, s[76:77]\n" \
  "global_load_dword v171, v171, s[76:77] offset:128\n" \
  "global_load_dword v164, v172, s[76:77]\n" \
  "global_load_dword v172, v172, s[76:77] offset:128\n" \
  "global_load_dword v165, v173, s[76:77]\n" \
  "global_load_dword v173, v173, s[76:77] offset:128\n" \
  "global_load_dword v166, v174, s[76:77]\n" \
  "global_load_dword v174, v174, s[76:77] offset:128\n" \
  "global_load_dword v167, v175, s[76:77]\n" \
  "global_load_dword v175, v175, s[76:77] offset:128\n" \
  "v_mad_u32_u16 v240, s67, 20, %[c_s]\n" \
  "ds_read_b128 v[212:215], v240\n" \
  "v_mad_u32_u16 v238, s64, 64, %[c_a]\n" \
  "v_mad_u32_u16 v239, s64, 20, %[c_y]\n" \
  "ds_read_b64_tr_b16 v[216:217], v238\n" \
  "ds_read_b64_tr_b16 v[218:219], v238 offset:128\n" \
  "ds_read_b128 v[192:195], v239\n" \
  "ds_read_b128 v[196:199], v239 offset:32\n" \
  "v_mov_b64 v[64:65], 0\n" \
  "v_mov_b64 v[66:67], 0\n" \
  "v_mov_b64 v[68:69], 0\n" \
  "v_mov_b64 v[70:71], 0\n" \
  "v_mov_b64 v[72:73], 0\n" \
  "v_mov_b64 v[74:75], 0\n" \
  "v_mov_b64 v[76:77], 0\n" \
  "v_mov_b64 v[78:79], 0\n" \
  "v_mov_b64 v[80:81], 0\n" \
  "v_mov_b64 v[82:83], 0\n" \
  "v_mov_b64 v[84:85], 0\n" \
  "v_mov_b64 v[86:87], 0\n" \
  "v_mov_b64 v[88:89], 0\n" \
  "v_mov_b64 v[90:91], 0\n" \
  "v_mov_b64 v[92:93], 0\n" \
  "v_mov_b64 v[94:95], 0\n" \
  "v_mov_b64 v[96:97], 0\n" \
  "v_mov_b64 v[98:99], 0\n" \
  "v_mov_b64 v[100:101], 0\n" \
  "v_mov_b64 v[102:103], 0\n" \
  "v_mov_b64 v[104:105], 0\n" \
  "v_mov_b64 v[106:107], 0\n" \
  "v_mov_b64 v[108:109], 0\n" \
  "v_mov_b64 v[110:111], 0\n" \
  "v_mov_b64 v[112:113], 0\n" \
  "v_mov_b64 v[114:115], 0\n" \
  "v_mov_b64 v[116:117], 0\n" \
  "v_mov_b64 v[118:119], 0\n" \
  "v_mov_b64 v[120:121], 0\n" \
  "v_mov_b64 v[122:123], 0\n" \
  "v_mov_b64 v[124:125], 0\n" \
  "v_mov_b64 v[126:127], 0\n" \
  "s_waitcnt lgkmcnt(0)\n" \
  "v_pk_mul_f16 v192, v192, v216\n" \
  "v_pk_mul_f16 v193, v193, v217\n" \
  "v_pk_mul_f16 v194, v194, v218\n" \
  "v_pk_mul_f16 v195, v195, v219\n" \
  "v_pk_mul_f16 v196, v196, v216\n" \
  "v_pk_mul_f16 v197, v197, v217\n" \
  "v_pk_mul_f16 v198, v198, v218\n" \
  "v_pk_mul_f16 v199, v199, v219\n" \
  "v_mad_u32_u16 v238, s65, 64, %[c_a]\n" \
  "v_mad_u32_u16 v239, s65, 20, %[c_y]\n" \
  "s_bitcmp1_b32 s67, 16\n" \
  "s_cselect_b64 s[76:77], s[78:79], %[xp]\n" \
  "v_mad_u32_u16 v184, v212, s84, %[c_x]\n" \
  "s_waitcnt vmcnt(32)\n" \
  "s_mov_b32 s91, 0\n" \
  "s_setprio 1\n" \
  ".p2align 6\n" \
  "Lloop_%=:\n" \
  "v_smfmac_f32_32x32x32_f16 v[0:15], v[192:195], v[128:135], %[ie]\n" \
  "global_load_dword v176, v184, s[76:77]\n" \
  "global_load_dword v184, v184, s[76:77] offset:128\n" \
  "ds_read_b64_tr_b16 v[216:217], v238\n" \
  "v_mad_u32_u16 v185, v212, s84, %[c_x] op_sel:[1,0,0,0]\n" \
  "v_readlane_b32 s64, v241, s80\n" \
  "ds_read_b64_tr_b16 v[218:219], v238 offset:128\n" \
  "s_add_u32 s80, s80, 1\n" \
  "s_nop 0\n" \
  "v_smfmac_f32_32x32x32_f16 v[16:31], v[192:195], v[128:135], %[io]\n" \
  "global_load_dword v177, v185, s[76:77]\n" \
  "global_load_dword v185, v185, s[76:77] offset:128\n" \
  "ds_read_b128 v[200:203], v239\n" \
  "v_mad_u32_u16 v186, v213, s84, %[c_x]\n" \
  "v_mad_u32_u16 v240, s64, 20, %[c_s]\n" \
  "ds_read_b128 v[204:207], v239 offset:32\n" \
  "v_smfmac_f32_32x32x32_f16 v[32:47], v[196:199], v[128:135], %[ie]\n" \
  "global_load_dword v178, v186, s[76:77]\n" \
  "global_load_dword v186, v186, s[76:77] offset:128\n" \
  "ds_read_b128 v[208:211], v240\n" \
  "v_mad_u32_u16 v187, v213, s84, %[c_x] op_sel:[1,0,0,0]\n" \
  "v_mad_u32_u16 v238, s66, 64, %[c_a]\n" \
  "v_mad_u32_u16 v239, s66, 20, %[c_y]\n" \
  "v_smfmac_f32_32x32x32_f16 v[48:63], v[196:199], v[128:135], %[io]\n" \
  "global_load_dword v179, v187, s[76:77]\n" \
  "global_load_dword v187, v187, s[76:77] offset:128\n" \
  "s_waitcnt lgkmcnt(2)\n" \
  "s_nop 0\n" \
  "v_mad_u32_u16 v188, v214, s84, %[c_x]\n" \
  "v_pk_mul_f16 v200, v200, v216\n" \
  "v_pk_mul_f16 v201, v201, v217\n" \
  "v_smfmac_f32_32x32x32_f16 v[64:79], v[192:195], v[136:143], %[ie]\n" \
  "global_load_dword v180, v188, s[76:77]\n" \
  "global_load_dword v188, v188, s[76:77] offset:128\n" \
  "v_mad_u32_u16 v189, v214, s84, %[c_x] op_sel:[1,0,0,0]\n" \
  "v_pk_mul_f16 v202, v202, v218\n" \
  "v_pk_mul_f16 v203, v203, v219\n" \
  "v_smfmac_f32_32x32x32_f16 v[80:95], v[192:195], v[136:143], %[io]\n" \
  "global_load_dword v181, v189, s[76:77]\n" \
  "global_load_dword v189, v189, s[76:77] offset:128\n" \
  "s_waitcnt lgkmcnt(0)\n" \
  "s_nop 0\n" \
  "v_mad_u32_u16 v190, v215, s84, %[c_x]\n" \
  "v_pk_mul_f16 v204, v204, v216\n" \
  "v_pk_mul_f16 v205, v205, v217\n" \
  "v_smfmac_f32_32x32x32_f16 v[96:111], v[196:199], v[136:143], %[ie]\n" \
  "global_load_dword v182, v190, s[76:77]\n" \
  "global_load_dword v190, v190, s[76:77] offset:128\n" \
  "v_mad_u32_u16 v191, v215, s84, %[c_x] op_sel:[1,0,0,0]\n" \
  "v_pk_mul_f16 v206, v206, v218\n" \
  "v_pk_mul_f16 v207, v207, v219\n" \
  "v_smfmac_f32_32x32x32_f16 v[112:127], v[196:199], v[136:143], %[io]\n" \
  "global_load_dword v183, v191, s[76:77]\n" \
  "global_load_dword v191, v191, s[76:77] offset:128\n" \
  "s_bitcmp1_b32 s73, 0\n" \
  "s_cbranch_scc1 Lpc0_%=\n" \
  "Lpr0_%=:\n" \
  "s_bitcmp1_b32 s64, 16\n" \
  "s_cselect_b64 s[76:77], s[78:79], %[xp]\n" \
  "s_bitcmp1_b32 s65, 17\n" \
  "s_cbranch_scc1 Lepi0_%=\n" \
  "Lret0_%=:\n" \
  "v_mad_u32_u16 v136, v208, s84, %[c_x]\n" \
  "s_waitcnt vmcnt(32)\n" \
  "Lwr0_%=:\n" \
  "s_nop 0\n" \
  "v_smfmac_f32_32x32x32_f16 v[0:15], v[200:203], v[144:151], %[ie]\n" \
  "global_load_dword v128, v136, s[76:77]\n" \
  "global_load_dword v136, v136, s[76:77] offset:128\n" \
  "ds_read_b64_tr_b16 v[216:217], v238\n" \
  "v_mad_u32_u16 v137, v208, s84, %[c_x] op_sel:[1,0,0,0]\n" \
  "v_readlane_b32 s65, v241, s80\n" \
  "ds_read_b64_tr_b16 v[218:219], v238 offset:128\n" \
  "s_add_u32 s80, s80, 1\n" \
  "s_nop 0\n" \
  "v_smfmac_f32_32x32x32_f16 v[16:31], v[200:203], v[144:151], %[io]\n" \
  "global_load_dword v129, v137, s[76:77]\n" \
  "global_load_dword v137, v137, s[76:77] offset:128\n" \
  "ds_read_b128 v[192:195], v239\n" \
  "v_mad_u32_u16 v138, v209, s84, %[c_x]\n" \
  "v_mad_u32_u16 v240, s65, 20, %[c_s]\n" \
  "ds_read_b128 v[196:199], v239 offset:32\n" \
  "v_smfmac_f32_32x32x32_f16 v[32:47], v[204:207], v[144:151], %[ie]\n" \
  "global_load_dword v130, v138, s[76:77]\n" \
  "global_load_dword v138, v138, s[76:77] offset:128\n" \
  "ds_read_b128 v[212:215], v240\n" \
  "v_mad_u32_u16 v139, v209, s84, %[c_x] op_sel:[1,0,0,0]\n" \
  "v_mad_u32_u16 v238, s67, 64, %[c_a]\n" \
  "v_mad_u32_u16 v239, s67, 20, %[c_y]\n" \
  "v_smfmac_f32_32x32x32_f16 v[48:63], v[204:207], v[144:151], %[io]\n" \
  "global_load_dword v131, v139, s[76:77]\n" \
  "global_load_dword v139, v139, s[76:77] offset:128\n" \
  "s_waitcnt lgkmcnt(2)\n" \
  "s_nop 0\n" \
  "v_mad_u32_u16 v140, v210, s84, %[c_x]\n" \
  "v_pk_mul_f16 v192, v192, v216\n" \
  "v_pk_mul_f16 v193, v193, v217\n" \
  "v_smfmac_f32_32x32x32_f16 v[64:79], v[200:203], v[152:159], %[ie]\n" \
  "global_load_dword v132, v140, s[76:77]\n" \
  "global_load_dword v140, v140, s[76:77] offset:128\n" \
  "v_mad_u32_u16 v141, v210, s84, %[c_x] op_sel:[1,0,0,0]\n" \
  "v_pk_mul_f16 v194, v194, v218\n" \
  "v_pk_mul_f16 v195, v195, v219\n" \
  "v_smfmac_f32_32x32x32_f16 v[80:95], v[200:203], v[152:159], %[io]\n" \
  "global_load_dword v133, v141, s[76:77]\n" \
  "global_load_dword v141, v141, s[76:77] offset:128\n" \
  "s_waitcnt lgkmcnt(0)\n" \
  "s_nop 0\n" \
  "v_mad_u32_u16 v142, v211, s84, %[c_x]\n" \
  "v_pk_mul_f16 v196, v196, v216\n" \
  "v_pk_mul_f16 v197, v197, v217\n" \
  "v_smfmac_f32_32x32x32_f16 v[96:111], v[204:207], v[152:159], %[ie]\n" \
  "global_load_dword v134, v142, s[76:77]\n" \
  "global_load_dword v142, v142, s[76:77] offset:128\n" \
  "v_mad_u32_u16 v143, v211, s84, %[c_x] op_sel:[1,0,0,0]\n" \
  "v_pk_mul_f16 v198, v198, v218\n" \
  "v_pk_mul_f16 v199, v199, v219\n" \
  "v_smfmac_f32_32x32x32_f16 v[112:127], v[204:207], v[152:159], %[io]\n" \
  "global_load_dword v135, v143, s[76:77]\n" \
  "global_load_dword v143, v143, s[76:77] offset:128\n" \
  "s_bitcmp1_b32 s73, 0\n" \
  "s_cbranch_scc1 Lpc1_%=\n" \
  "Lpr1_%=:\n" \
  "s_bitcmp1_b32 s65, 16\n" \
  "s_cselect_b64 s[76:77], s[78:79], %[xp]\n" \
  "s_bitcmp1_b32 s66, 17\n" \
  "s_cbranch_scc1 Lepi1_%=\n" \
  "Lret1_%=:\n" \
  "v_mad_u32_u16 v152, v212, s84, %[c_x]\n" \
  "s_waitcnt vmcnt(32)\n" \
  "Lwr1_%=:\n" \
  "s_nop 0\n" \
  "v_smfmac_f32_32x32x32_f16 v[0:15], v[192:195], v[160:167], %[ie]\n" \
  "global_load_dword v144, v152, s[76:77]\n" \
  "global_load_dword v152, v152, s[76:77] offset:128\n" \
  "ds_read_b64_tr_b16 v[216:217], v238\n" \
  "v_mad_u32_u16 v153, v212, s84, %[c_x] op_sel:[1,0,0,0]\n" \
  "v_readlane_b32 s66, v241, s80\n" \
  "ds_read_b64_tr_b16 v[218:219], v238 offset:128\n" \
  "s_add_u32 s80, s80, 1\n" \
  "s_nop 0\n" \
  "v_smfmac_f32_32x32x32_f16 v[16:31], v[192:195], v[160:167], %[io]\n" \
  "global_load_dword v145, v153, s[76:77]\n" \
  "global_load_dword v153, v153, s[76:77] offset:128\n" \
  "ds_read_b128 v[200:203], v239\n" \
  "v_mad_u32_u16 v154, v213, s84, %[c_x]\n" \
  "v_mad_u32_u16 v240, s66, 20, %[c_s]\n" \
  "ds_read_b128 v[204:207], v239 offset:32\n" \
  "v_smfmac_f32_32x32x32_f16 v[32:47], v[196:199], v[160:167], %[ie]\n" \
  "global_load_dword v146, v154, s[76:77]\n" \
  "global_load_dword v154, v154, s[76:77] offset:128\n" \
  "ds_read_b128 v[208:211], v240\n" \
  "v_mad_u32_u16 v155, v213, s84, %[c_x] op_sel:[1,0,0,0]\n" \
  "v_mad_u32_u16 v238, s64, 64, %[c_a]\n" \
  "v_mad_u32_u16 v239, s64, 20, %[c_y]\n" \
  "v_smfmac_f32_32x32x32_f16 v[48:63], v[196:199], v[160:167], %[io]\n" \
  "global_load_dword v147, v155, s[76:77]\n" \
  "global_load_dword v155, v155, s[76:77] offset:128\n" \
  "s_waitcnt lgkmcnt(2)\n" \
  "s_nop 0\n" \
  "v_mad_u32_u16 v156, v214, s84, %[c_x]\n" \
  "v_pk_mul_f16 v200, v200, v216\n" \
  "v_pk_mul_f16 v201, v201, v217\n" \
  "v_smfmac_f32_32x32x32_f16 v[64:79], v[192:195], v[168:175], %[ie]\n" \
  "global_load_dword v148, v156, s[76:77]\n" \
  "global_load_dword v156, v156, s[76:77] offset:128\n" \
  "v_mad_u32_u16 v157, v214, s84, %[c_x] op_sel:[1,0,0,0]\n" \
  "v_pk_mul_f16 v202, v202, v218\n" \
  "v_pk_mul_f16 v203, v203, v219\n" \
  "v_smfmac_f32_32x32x32_f16 v[80:95], v[192:195], v[168:175], %[io]\n" \
  "global_load_dword v149, v157, s[76:77]\n" \
  "global_load_dword v157, v157, s[76:77] offset:128\n" \
  "s_waitcnt lgkmcnt(0)\n" \
  "s_nop 0\n" \
  "v_mad_u32_u16 v158, v215, s84, %[c_x]\n" \
  "v_pk_mul_f16 v204, v204, v216\n" \
  "v_pk_mul_f16 v205, v205, v217\n" \
  "v_smfmac_f32_32x32x32_f16 v[96:111], v[196:199], v[168:175], %[ie]\n" \
  "global_load_dword v150, v158, s[76:77]\n" \
  "global_load_dword v158, v158, s[76:77] offset:128\n" \
  "v_mad_u32_u16 v159, v215, s84, %[c_x] op_sel:[1,0,0,0]\n" \
  "v_pk_mul_f16 v206, v206, v218\n" \
  "v_pk_mul_f16 v207, v207, v219\n" \
  "v_smfmac_f32_32x32x32_f16 v[112:127], v[196:199], v[168:175], %[io]\n" \
  "global_load_dword v151, v159, s[76:77]\n" \
  "global_load_dword v159, v159, s[76:77] offset:128\n" \
  "s_bitcmp1_b32 s73, 0\n" \
  "s_cbranch_scc1 Lpc2_%=\n" \
  "Lpr2_%=:\n" \
  "s_bitcmp1_b32 s66, 16\n" \
  "s_cselect_b64 s[76:77], s[78:79], %[xp]\n" \
  "s_bitcmp1_b32 s67, 17\n" \
  "s_cbranch_scc1 Lepi2_%=\n" \
  "Lret2_%=:\n" \
  "v_mad_u32_u16 v168, v208, s84, %[c_x]\n" \
  "s_waitcnt vmcnt(32)\n" \
  "Lwr2_%=:\n" \
  "s_nop 0\n" \
  "v_smfmac_f32_32x32x32_f16 v[0:15], v[200:203], v[176:183], %[ie]\n" \
  "global_load_dword v160, v168, s[76:77]\n" \
  "global_load_dword v168, v168, s[76:77] offset:128\n" \
  "ds_read_b64_tr_b16 v[216:217], v238\n" \
  "v_mad_u32_u16 v169, v208, s84, %[c_x] op_sel:[1,0,0,0]\n" \
  "v_readlane_b32 s67, v241, s80\n" \
  "ds_read_b64_tr_b16 v[218:219], v238 offset:128\n" \
  "s_add_u32 s80, s80, 1\n" \
  "s_nop 0\n" \
  "v_smfmac_f32_32x32x32_f16 v[16:31], v[200:203], v[176:183], %[io]\n" \
  "global_load_dword v161, v169, s[76:77]\n" \
  "global_load_dword v169, v169, s[76:77] offset:128\n" \
  "ds_read_b128 v[192:195], v239\n" \
  "v_mad_u32_u16 v170, v209, s84, %[c_x]\n" \
  "v_mad_u32_u16 v240, s67, 20, %[c_s]\n" \
  "ds_read_b128 v[196:199], v239 offset:32\n" \
  "v_smfmac_f32_32x32x32_f16 v[32:47], v[204:207], v[176:183], %[ie]\n" \
  "global_load_dword v162, v170, s[76:77]\n" \
  "global_load_dword v170, v170, s[76:77] offset:128\n" \
  "ds_read_b128 v[212:215], v240\n" \
  "v_mad_u32_u16 v171, v209, s84, %[c_x] op_sel:[1,0,0,0]\n" \
  "v_mad_u32_u16 v238, s65, 64, %[c_a]\n" \
  "v_mad_u32_u16 v239, s65, 20, %[c_y]\n" \
  "v_smfmac_f32_32x32x32_f16 v[48:63], v[204:207], v[176:183], %[io]\n" \
  "global_load_dword v163, v171, s[76:77]\n" \
  "global_load_dword v171, v171, s[76:77] offset:128\n" \
  "s_waitcnt lgkmcnt(2)\n" \
  "s_nop 0\n" \
  "v_mad_u32_u16 v172, v210, s84, %[c_x]\n" \
  "v_pk_mul_f16 v192, v192, v216\n" \
  "v_pk_mul_f16 v193, v193, v217\n" \
  "v_smfmac_f32_32x32x32_f16 v[64:79], v[200:203], v[184:191], %[ie]\n" \
  "global_load_dword v164, v172, s[76:77]\n" \
  "global_load_dword v172, v172, s[76:77] offset:128\n" \
  "v_mad_u32_u16 v173, v210, s84, %[c_x] op_sel:[1,0,0,0]\n" \
  "v_pk_mul_f16 v194, v194, v218\n" \
  "v_pk_mul_f16 v195, v195, v219\n" \
  "v_smfmac_f32_32x32x32_f16 v[80:95], v[200:203], v[184:191], %[io]\n" \
  "global_load_dword v165, v173, s[76:77]\n" \
  "global_load_dword v173, v173, s[76:77] offset:128\n" \
  "s_waitcnt lgkmcnt(0)\n" \
  "s_nop 0\n" \
  "v_mad_u32_u16 v174, v211, s84, %[c_x]\n" \
  "v_pk_mul_f16 v196, v196, v216\n" \
  "v_pk_mul_f16 v197, v197, v217\n" \
  "v_smfmac_f32_32x32x32_f16 v[96:111], v[204:207], v[184:191], %[ie]\n" \
  "global_load_dword v166, v174, s[76:77]\n" \
  "global_load_dword v174, v174, s[76:77] offset:128\n" \
  "v_mad_u32_u16 v175, v211, s84, %[c_x] op_sel:[1,0,0,0]\n" \
  "v_pk_mul_f16 v198, v198, v218\n" \
  "v_pk_mul_f16 v199, v199, v219\n" \
  "v_smfmac_f32_32x32x32_f16 v[112:127], v[204:207], v[184:191], %[io]\n" \
  "global_load_dword v167, v175, s[76:77]\n" \
  "global_load_dword v175, v175, s[76:77] offset:128\n" \
  "s_bitcmp1_b32 s73, 0\n" \
  "s_cbranch_scc1 Lpc3_%=\n" \
  "Lpr3_%=:\n" \
  "s_bitcmp1_b32 s67, 16\n" \
  "s_cselect_b64 s[76:77], s[78:79], %[xp]\n" \
  "s_bitcmp1_b32 s64, 17\n" \
  "s_cbranch_scc1 Lepi3_%=\n" \
  "Lret3_%=:\n" \
  "v_mad_u32_u16 v184, v212, s84, %[c_x]\n" \
  "s_waitcnt vmcnt(32)\n" \
  "Lwr3_%=:\n" \
  "s_nop 0\n" \
  "s_cmp_lg_u32 s80, 64\n" \
  "s_cbranch_scc1 Lloop_%=\n" \
  "s_add_u32 s89, s89, 256\n" \
  "v_mbcnt_lo_u32_b32 v241, -1, 0\n" \
  "v_mbcnt_hi_u32_b32 v241, -1, v241\n" \
  "v_lshl_add_u32 v241, v241, 2, s89\n" \
  "ds_read_b32 v241, v241\n" \
  "s_mov_b32 s80, 0\n" \
  "s_waitcnt lgkmcnt(0)\n" \
  "s_branch Lloop_%=\n" \
  ".p2align 3\n" \
  "Lepi0_%=:\n" \
  "s_setprio 0\n" \
  "s_bitcmp1_b32 s65, 18\n" \
  "s_cbranch_scc1 Lep10_%=\n" \
  "s_cmp_eq_u32 %[gen], 0\n" \
  "s_cbranch_scc1 Lg00_%=\n" \
  "ds_read_b128 v[128:131], v232 offset:8704\n" \
  "ds_read_b128 v[132:135], v232 offset:9216\n" \
  "ds_read_b128 v[136:139], v232 offset:9728\n" \
  "ds_read_b128 v[140:143], v232 offset:10240\n" \
  "s_waitcnt lgkmcnt(3)\n" \
  "v_fma_mix_f32 v230, v64, v128, 0 op_sel:[0,0,0] op_sel_hi:[0,1,0]\n" \
  "v_fma_mix_f32 v231, v80, v128, 0 op_sel:[0,1,0] op_sel_hi:[0,1,0]\n" \
  "v_fma_mix_f32 v230, v65, v129, v230 op_sel:[0,0,0] op_sel_hi:[0,1,0]\n" \
  "v_fma_mix_f32 v231, v81, v129, v231 op_sel:[0,1,0] op_sel_hi:[0,1,0]\n" \
  "v_fma_mix_f32 v230, v66, v130, v230 op_sel:[0,0,0] op_sel_hi:[0,1,0]\n" \
  "v_fma_mix_f32 v231, v82, v130, v231 op_sel:[0,1,0] op_sel_hi:[0,1,0]\n" \
  "v_fma_mix_f32 v230, v67, v131, v230 op_sel:[0,0,0] op_sel_hi:[0,1,0]\n" \
  "v_fma_mix_f32 v231, v83, v131, v231 op_sel:[0,1,0] op_sel_hi:[0,1,0]\n" \
  "s_waitcnt lgkmcnt(2)\n" \
  "v_fma_mix_f32 v230, v68, v132, v230 op_sel:[0,0,0] op_sel_hi:[0,1,0]\n" \
  "v_fma_mix_f32 v231, v84, v132, v231 op_sel:[0,1,0] op_sel_hi:[0,1,0]\n" \
  "v_fma_mix_f32 v230, v69, v133, v230 op_sel:[0,0,0] op_sel_hi:[0,1,0]\n" \
  "v_fma_mix_f32 v231, v85, v133, v231 op_sel:[0,1,0] op_sel_hi:[0,1,0]\n" \
  "v_fma_mix_f32 v230, v70, v134, v230 op_sel:[0,0,0] op_sel_hi:[0,1,0]\n" \
  "v_fma_mix_f32 v231, v86, v134, v231 op_sel:[0,1,0] op_sel_hi:[0,1,0]\n" \
  "v_fma_mix_f32 v230, v71, v135, v230 op_sel:[0,0,0] op_sel_hi:[0,1,0]\n" \
  "v_fma_mix_f32 v231, v87, v135, v231 op_sel:[0,1,0] op_sel_hi:[0,1,0]\n" \
  "s_waitcnt lgkmcnt(1)\n" \
  "v_fma_mix_f32 v230, v72, v136, v230 op_sel:[0,0,0] op_sel_hi:[0,1,0]\n" \
  "v_fma_mix_f32 v231, v88, v136, v231 op_sel:[0,1,0] op_sel_hi:[0,1,0]\n" \
  "v_fma_mix_f32 v230, v73, v137, v230 op_sel:[0,0,0] op_sel_hi:[0,1,0]\n" \
  "v_fma_mix_f32 v231, v89, v137, v231 op_sel:[0,1,0] op_sel_hi:[0,1,0]\n" \
  "v_fma_mix_f32 v230, v74, v138, v230 op_sel:[0,0,0] op_sel_hi:[0,1,0]\n" \
  "v_fma_mix_f32 v231, v90, v138, v231 op_sel:[0,1,0] op_sel_hi:[0,1,0]\n" \
  "v_fma_mix_f32 v230, v75, v139, v230 op_sel:[0,0,0] op_sel_hi:[0,1,0]\n" \
  "v_fma_mix_f32 v231, v91, v139, v231 op_sel:[0,1,0] op_sel_hi:[0,1,0]\n" \
  "s_waitcnt lgkmcnt(0)\n" \
  "v_fma_mix_f32 v230, v76, v140, v230 op_sel:[0,0,0] op_sel_hi:[0,1,0]\n" \
  "v_fma_mix_f32 v231, v92, v140, v231 op_sel:[0,1,0] op_sel_hi:[0,1,0]\n" \
  "v_fma_mix_f32 v230, v77, v141, v230 op_sel:[0,0,0] op_sel_hi:[0,1,0]\n" \
  "v_fma_mix_f32 v231, v93, v141, v231 op_sel:[0,1,0] op_sel_hi:[0,1,0]\n" \
  "v_fma_mix_f32 v230, v78, v142, v230 op_sel:[0,0,0] op_sel_hi:[0,1,0]\n" \
  "v_fma_mix_f32 v231, v94, v142, v231 op_sel:[0,1,0] op_sel_hi:[0,1,0]\n" \
  "v_fma_mix_f32 v230, v79, v143, v230 op_sel:[0,0,0] op_sel_hi:[0,1,0]\n" \
  "v_fma_mix_f32 v231, v95, v143, v231 op_sel:[0,1,0] op_sel_hi:[0,1,0]\n" \
  "s_add_u32 s94, s92, 0x2000000\n" \
  "s_addc_u32 s95, s93, 0\n" \
  "global_store_dwordx2 v233, v[230:231], s[94:95]\n" \
  "Lg00_%=:\n" \
  "ds_read_b128 v[128:131], v233 offset:512\n" \
  "ds_read_b128 v[132:135], v233 offset:1024\n" \
  "ds_read_b128 v[136:139], v233 offset:1536\n" \
  "ds_read_b128 v[140:143], v233 offset:2048\n" \
  "ds_read_b128 v[192:195], v232 offset:4608\n" \
  "ds_read_b128 v[196:199], v232 offset:5120\n" \
  "s_waitcnt lgkmcnt(5)\n" \
  "v_fma_mix_f32 v216, v0, v128, 0 op_sel:[0,0,0] op_sel_hi:[0,1,0]\n" \
  "v_fma_mix_f32 v217, v16, v128, 0 op_sel:[0,1,0] op_sel_hi:[0,1,0]\n" \
  "v_fma_mix_f32 v216, v1, v129, v216 op_sel:[0,0,0] op_sel_hi:[0,1,0]\n" \
  "v_fma_mix_f32 v217, v17, v129, v217 op_sel:[0,1,0] op_sel_hi:[0,1,0]\n" \
  "v_fma_mix_f32 v216, v2, v130, v216 op_sel:[0,0,0] op_sel_hi:[0,1,0]\n" \
  "v_fma_mix_f32 v217, v18, v130, v217 op_sel:[0,1,0] op_sel_hi:[0,1,0]\n" \
  "v_fma_mix_f32 v216, v3, v131, v216 op_sel:[0,0,0] op_sel_hi:[0,1,0]\n" \
  "v_fma_mix_f32 v217, v19, v131, v217 op_sel:[0,1,0] op_sel_hi:[0,1,0]\n" \
  "ds_read_b128 v[128:131], v232 offset:5632\n" \
  "s_waitcnt lgkmcnt(5)\n" \
  "v_fma_mix_f32 v216, v4, v132, v216 op_sel:[0,0,0] op_sel_hi:[0,1,0]\n" \
  "v_fma_mix_f32 v217, v20, v132, v217 op_sel:[0,1,0] op_sel_hi:[0,1,0]\n" \
  "v_fma_mix_f32 v216, v5, v133, v216 op_sel:[0,0,0] op_sel_hi:[0,1,0]\n" \
  "v_fma_mix_f32 v217, v21, v133, v217 op_sel:[0,1,0] op_sel_hi:[0,1,0]\n" \
  "v_fma_mix_f32 v216, v6, v134, v216 op_sel:[0,0,0] op_sel_hi:[0,1,0]\n" \
  "v_fma_mix_f32 v217, v22, v134, v217 op_sel:[0,1,0] op_sel_hi:[0,1,0]\n" \
  "v_fma_mix_f32 v216, v7, v135, v216 op_sel:[0,0,0] op_sel_hi:[0,1,0]\n" \
  "v_fma_mix_f32 v217, v23, v135, v217 op_sel:[0,1,0] op_sel_hi:[0,1,0]\n" \
  "ds_read_b128 v[132:135], v232 offset:6144\n" \
  "s_waitcnt lgkmcnt(5)\n" \
  "v_fma_mix_f32 v216, v8, v136, v216 op_sel:[0,0,0] op_sel_hi:[0,1,0]\n" \
  "v_fma_mix_f32 v217, v24, v136, v217 op_sel:[0,1,0] op_sel_hi:[0,1,0]\n" \
  "v_fma_mix_f32 v216, v9, v137, v216 op_sel:[0,0,0] op_sel_hi:[0,1,0]\n" \
  "v_fma_mix_f32 v217, v25, v137, v217 op_sel:[0,1,0] op_sel_hi:[0,1,0]\n" \
  "v_fma_mix_f32 v216, v10, v138, v216 op_sel:[0,0,0] op_sel_hi:[0,1,0]\n" \
  "v_fma_mix_f32 v217, v26, v138, v217 op_sel:[0,1,0] op_sel_hi:[0,1,0]\n" \
  "v_fma_mix_f32 v216, v11, v139, v216 op_sel:[0,0,0] op_sel_hi:[0,1,0]\n" \
  "v_fma_mix_f32 v217, v27, v139, v217 op_sel:[0,1,0] op_sel_hi:[0,1,0]\n" \
  "ds_read_b128 v[136:139], v234 offset:2560\n" \
  "s_waitcnt lgkmcnt(5)\n" \
  "v_fma_mix_f32 v216, v12, v140, v216 op_sel:[0,0,0] op_sel_hi:[0,1,0]\n" \
  "v_fma_mix_f32 v217, v28, v140, v217 op_sel:[0,1,0] op_sel_hi:[0,1,0]\n" \
  "v_fma_mix_f32 v216, v13, v141, v216 op_sel:[0,0,0] op_sel_hi:[0,1,0]\n" \
  "v_fma_mix_f32 v217, v29, v141, v217 op_sel:[0,1,0] op_sel_hi:[0,1,0]\n" \
  "v_fma_mix_f32 v216, v14, v142, v216 op_sel:[0,0,0] op_sel_hi:[0,1,0]\n" \
  "v_fma_mix_f32 v217, v30, v142, v217 op_sel:[0,1,0] op_sel_hi:[0,1,0]\n" \
  "v_fma_mix_f32 v216, v15, v143, v216 op_sel:[0,0,0] op_sel_hi:[0,1,0]\n" \
  "v_fma_mix_f32 v217, v31, v143, v217 op_sel:[0,1,0] op_sel_hi:[0,1,0]\n" \
  "ds_read_b128 v[140:143], v234 offset:3072\n" \
  "v_mfma_f32_32x32x4_2b_f16 v[0:31], v[228:229], v[228:229], 0\n" \
  "s_waitcnt lgkmcnt(5)\n" \
  "v_fma_mix_f32 v218, v32, v192, 0 op_sel:[0,0,0] op_sel_hi:[0,1,0]\n" \
  "v_fma_mix_f32 v219, v48, v192, 0 op_sel:[0,1,0] op_sel_hi:[0,1,0]\n" \
  "v_fma_mix_f32 v218, v33, v193, v218 op_sel:[0,0,0] op_sel_hi:[0,1,0]\n" \
  "v_fma_mix_f32 v219, v49, v193, v219 op_sel:[0,1,0] op_sel_hi:[0,1,0]\n" \
  "v_fma_mix_f32 v218, v34, v194, v218 op_sel:[0,0,0] op_sel_hi:[0,1,0]\n" \
  "v_fma_mix_f32 v219, v50, v194, v219 op_sel:[0,1,0] op_sel_hi:[0,1,0]\n" \
  "v_fma_mix_f32 v218, v35, v195, v218 op_sel:[0,0,0] op_sel_hi:[0,1,0]\n" \
  "v_fma_mix_f32 v219, v51, v195, v219 op_sel:[0,1,0] op_sel_hi:[0,1,0]\n" \
  "ds_read_b128 v[192:195], v234 offset:3584\n" \
  "s_waitcnt lgkmcnt(5)\n" \
  "v_fma_mix_f32 v218, v36, v196, v218 op_sel:[0,0,0] op_sel_hi:[0,1,0]\n" \
  "v_fma_mix_f32 v219, v52, v196, v219 op_sel:[0,1,0] op_sel_hi:[0,1,0]\n" \
  "v_fma_mix_f32 v218, v37, v197, v218 op_sel:[0,0,0] op_sel_hi:[0,1,0]\n" \
  "v_fma_mix_f32 v219, v53, v197, v219 op_sel:[0,1,0] op_sel_hi:[0,1,0]\n" \
  "v_fma_mix_f32 v218, v38, v198, v218 op_sel:[0,0,0] op_sel_hi:[0,1,0]\n" \
  "v_fma_mix_f32 v219, v54, v198, v219 op_sel:[0,1,0] op_sel_hi:[0,1,0]\n" \
  "v_fma_mix_f32 v218, v39, v199, v218 op_sel:[0,0,0] op_sel_hi:[0,1,0]\n" \
  "v_fma_mix_f32 v219, v55, v199, v219 op_sel:[0,1,0] op_sel_hi:[0,1,0]\n" \
  "ds_read_b128 v[196:199], v234 offset:4096\n" \
  "s_waitcnt lgkmcnt(5)\n" \
  "v_fma_mix_f32 v218, v40, v128, v218 op_sel:[0,0,0] op_sel_hi:[0,1,0]\n" \
  "v_fma_mix_f32 v219, v56, v128, v219 op_sel:[0,1,0] op_sel_hi:[0,1,0]\n" \
  "v_fma_mix_f32 v218, v41, v129, v218 op_sel:[0,0,0] op_sel_hi:[0,1,0]\n" \
  "v_fma_mix_f32 v219, v57, v129, v219 op_sel:[0,1,0] op_sel_hi:[0,1,0]\n" \
  "v_fma_mix_f32 v218, v42, v130, v218 op_sel:[0,0,0] op_sel_hi:[0,1,0]\n" \
  "v_fma_mix_f32 v219, v58, v130, v219 op_sel:[0,1,0] op_sel_hi:[0,1,0]\n" \
  "v_fma_mix_f32 v218, v43, v131, v218 op_sel:[0,0,0] op_sel_hi:[0,1,0]\n" \
  "v_fma_mix_f32 v219, v59, v131, v219 op_sel:[0,1,0] op_sel_hi:[0,1,0]\n" \
  "ds_read_b128 v[128:131], v232 offset:8704\n" \
  "s_waitcnt lgkmcnt(5)\n" \
  "v_fma_mix_f32 v218, v44, v132, v218 op_sel:[0,0,0] op_sel_hi:[0,1,0]\n" \
  "v_fma_mix_f32 v219, v60, v132, v219 op_sel:[0,1,0] op_sel_hi:[0,1,0]\n" \
  "v_fma_mix_f32 v218, v45, v133, v218 op_sel:[0,0,0] op_sel_hi:[0,1,0]\n" \
  "v_fma_mix_f32 v219, v61, v133, v219 op_sel:[0,1,0] op_sel_hi:[0,1,0]\n" \
  "v_fma_mix_f32 v218, v46, v134, v218 op_sel:[0,0,0] op_sel_hi:[0,1,0]\n" \
  "v_fma_mix_f32 v219, v62, v134, v219 op_sel:[0,1,0] op_sel_hi:[0,1,0]\n" \
  "v_fma_mix_f32 v218, v47, v135, v218 op_sel:[0,0,0] op_sel_hi:[0,1,0]\n" \
  "v_fma_mix_f32 v219, v63, v135, v219 op_sel:[0,1,0] op_sel_hi:[0,1,0]\n" \
  "ds_read_b128 v[132:135], v232 offset:9216\n" \
  "v_mfma_f32_32x32x4_2b_f16 v[32:63], v[228:229], v[228:229], 0\n" \
  "s_waitcnt lgkmcnt(5)\n" \
  "v_fma_mix_f32 v212, v64, v136, 0 op_sel:[0,0,0] op_sel_hi:[0,1,0]\n" \
  "v_fma_mix_f32 v213, v80, v136, 0 op_sel:[0,1,0] op_sel_hi:[0,1,0]\n" \
  "v_fma_mix_f32 v212, v65, v137, v212 op_sel:[0,0,0] op_sel_hi:[0,1,0]\n" \
  "v_fma_mix_f32 v213, v81, v137, v213 op_sel:[0,1,0] op_sel_hi:[0,1,0]\n" \
  "v_fma_mix_f32 v212, v66, v138, v212 op_sel:[0,0,0] op_sel_hi:[0,1,0]\n" \
  "v_fma_mix_f32 v213, v82, v138, v213 op_sel:[0,1,0] op_sel_hi:[0,1,0]\n" \
  "v_fma_mix_f32 v212, v67, v139, v212 op_sel:[0,0,0] op_sel_hi:[0,1,0]\n" \
  "v_fma_mix_f32 v213, v83, v139, v213 op_sel:[0,1,0] op_sel_hi:[0,1,0]\n" \
  "ds_read_b128 v[136:139], v232 offset:9728\n" \
  "s_waitcnt lgkmcnt(5)\n" \
  "v_fma_mix_f32 v212, v68, v140, v212 op_sel:[0,0,0] op_sel_hi:[0,1,0]\n" \
  "v_fma_mix_f32 v213, v84, v140, v213 op_sel:[0,1,0] op_sel_hi:[0,1,0]\n" \
  "v_fma_mix_f32 v212, v69, v141, v212 op_sel:[0,0,0] op_sel_hi:[0,1,0]\n" \
  "v_fma_mix_f32 v213, v85, v141, v213 op_sel:[0,1,0] op_sel_hi:[0,1,0]\n" \
  "v_fma_mix_f32 v212, v70, v142, v212 op_sel:[0,0,0] op_sel_hi:[0,1,0]\n" \
  "v_fma_mix_f32 v213, v86, v142, v213 op_sel:[0,1,0] op_sel_hi:[0,1,0]\n" \
  "v_fma_mix_f32 v212, v71, v143, v212 op_sel:[0,0,0] op_sel_hi:[0,1,0]\n" \
  "v_fma_mix_f32 v213, v87, v143, v213 op_sel:[0,1,0] op_sel_hi:[0,1,0]\n" \
  "ds_read_b128 v[140:143], v232 offset:10240\n" \
  "s_waitcnt lgkmcnt(5)\n" \
  "v_fma_mix_f32 v212, v72, v192, v212 op_sel:[0,0,0] op_sel_hi:[0,1,0]\n" \
  "v_fma_mix_f32 v213, v88, v192, v213 op_sel:[0,1,0] op_sel_hi:[0,1,0]\n" \
  "v_fma_mix_f32 v212, v73, v193, v212 op_sel:[0,0,0] op_sel_hi:[0,1,0]\n" \
  "v_fma_mix_f32 v213, v89, v193, v213 op_sel:[0,1,0] op_sel_hi:[0,1,0]\n" \
  "v_fma_mix_f32 v212, v74, v194, v212 op_sel:[0,0,0] op_sel_hi:[0,1,0]\n" \
  "v_fma_mix_f32 v213, v90, v194, v213 op_sel:[0,1,0] op_sel_hi:[0,1,0]\n" \
  "v_fma_mix_f32 v212, v75, v195, v212 op_sel:[0,0,0] op_sel_hi:[0,1,0]\n" \
  "v_fma_mix_f32 v213, v91, v195, v213 op_sel:[0,1,0] op_sel_hi:[0,1,0]\n" \
  "s_waitcnt lgkmcnt(4)\n" \
  "v_fma_mix_f32 v212, v76, v196, v212 op_sel:[0,0,0] op_sel_hi:[0,1,0]\n" \
  "v_fma_mix_f32 v213, v92, v196, v213 op_sel:[0,1,0] op_sel_hi:[0,1,0]\n" \
  "v_fma_mix_f32 v212, v77, v197, v212 op_sel:[0,0,0] op_sel_hi:[0,1,0]\n" \
  "v_fma_mix_f32 v213, v93, v197, v213 op_sel:[0,1,0] op_sel_hi:[0,1,0]\n" \
  "v_fma_mix_f32 v212, v78, v198, v212 op_sel:[0,0,0] op_sel_hi:[0,1,0]\n" \
  "v_fma_mix_f32 v213, v94, v198, v213 op_sel:[0,1,0] op_sel_hi:[0,1,0]\n" \
  "v_fma_mix_f32 v212, v79, v199, v212 op_sel:[0,0,0] op_sel_hi:[0,1,0]\n" \
  "v_fma_mix_f32 v213, v95, v199, v213 op_sel:[0,1,0] op_sel_hi:[0,1,0]\n" \
  "s_waitcnt lgkmcnt(3)\n" \
  "v_fma_mix_f32 v214, v96, v128, 0 op_sel:[0,0,0] op_sel_hi:[0,1,0]\n" \
  "v_fma_mix_f32 v215, v112, v128, 0 op_sel:[0,1,0] op_sel_hi:[0,1,0]\n" \
  "v_fma_mix_f32 v214, v97, v129, v214 op_sel:[0,0,0] op_sel_hi:[0,1,0]\n" \
  "v_fma_mix_f32 v215, v113, v129, v215 op_sel:[0,1,0] op_sel_hi:[0,1,0]\n" \
  "v_fma_mix_f32 v214, v98, v130, v214 op_sel:[0,0,0] op_sel_hi:[0,1,0]\n" \
  "v_fma_mix_f32 v215, v114, v130, v215 op_sel:[0,1,0] op_sel_hi:[0,1,0]\n" \
  "v_fma_mix_f32 v214, v99, v131, v214 op_sel:[0,0,0] op_sel_hi:[0,1,0]\n" \
  "v_fma_mix_f32 v215, v115, v131, v215 op_sel:[0,1,0] op_sel_hi:[0,1,0]\n" \
  "s_waitcnt lgkmcnt(2)\n" \
  "v_fma_mix_f32 v214, v100, v132, v214 op_sel:[0,0,0] op_sel_hi:[0,1,0]\n" \
  "v_fma_mix_f32 v215, v116, v132, v215 op_sel:[0,1,0] op_sel_hi:[0,1,0]\n" \
  "v_fma_mix_f32 v214, v101, v133, v214 op_sel:[0,0,0] op_sel_hi:[0,1,0]\n" \
  "v_fma_mix_f32 v215, v117, v133, v215 op_sel:[0,1,0] op_sel_hi:[0,1,0]\n" \
  "v_fma_mix_f32 v214, v102, v134, v214 op_sel:[0,0,0] op_sel_hi:[0,1,0]\n" \
  "v_fma_mix_f32 v215, v118, v134, v215 op_sel:[0,1,0] op_sel_hi:[0,1,0]\n" \
  "v_fma_mix_f32 v214, v103, v135, v214 op_sel:[0,0,0] op_sel_hi:[0,1,0]\n" \
  "v_fma_mix_f32 v215, v119, v135, v215 op_sel:[0,1,0] op_sel_hi:[0,1,0]\n" \
  "s_waitcnt lgkmcnt(1)\n" \
  "v_fma_mix_f32 v214, v104, v136, v214 op_sel:[0,0,0] op_sel_hi:[0,1,0]\n" \
  "v_fma_mix_f32 v215, v120, v136, v215 op_sel:[0,1,0] op_sel_hi:[0,1,0]\n" \
  "v_fma_mix_f32 v214, v105, v137, v214 op_sel:[0,0,0] op_sel_hi:[0,1,0]\n" \
  "v_fma_mix_f32 v215, v121, v137, v215 op_sel:[0,1,0] op_sel_hi:[0,1,0]\n" \
  "v_fma_mix_f32 v214, v106, v138, v214 op_sel:[0,0,0] op_sel_hi:[0,1,0]\n" \
  "v_fma_mix_f32 v215, v122, v138, v215 op_sel:[0,1,0] op_sel_hi:[0,1,0]\n" \
  "v_fma_mix_f32 v214, v107, v139, v214 op_sel:[0,0,0] op_sel_hi:[0,1,0]\n" \
  "v_fma_mix_f32 v215, v123, v139, v215 op_sel:[0,1,0] op_sel_hi:[0,1,0]\n" \
  "s_waitcnt lgkmcnt(0)\n" \
  "v_fma_mix_f32 v214, v108, v140, v214 op_sel:[0,0,0] op_sel_hi:[0,1,0]\n" \
  "v_fma_mix_f32 v215, v124, v140, v215 op_sel:[0,1,0] op_sel_hi:[0,1,0]\n" \
  "v_fma_mix_f32 v214, v109, v141, v214 op_sel:[0,0,0] op_sel_hi:[0,1,0]\n" \
  "v_fma_mix_f32 v215, v125, v141, v215 op_sel:[0,1,0] op_sel_hi:[0,1,0]\n" \
  "v_fma_mix_f32 v214, v110, v142, v214 op_sel:[0,0,0] op_sel_hi:[0,1,0]\n" \
  "v_fma_mix_f32 v215, v126, v142, v215 op_sel:[0,1,0] op_sel_hi:[0,1,0]\n" \
  "v_fma_mix_f32 v214, v111, v143, v214 op_sel:[0,0,0] op_sel_hi:[0,1,0]\n" \
  "v_fma_mix_f32 v215, v127, v143, v215 op_sel:[0,1,0] op_sel_hi:[0,1,0]\n" \
  "v_cvt_pk_bf16_f32 v220, v216, v217\n" \
  "v_cvt_pk_bf16_f32 v221, v218, v219\n" \
  "v_cvt_pk_bf16_f32 v222, v212, v213\n" \
  "v_cvt_pk_bf16_f32 v223, v214, v215\n" \
  "s_getpc_b64 s[70:71]\n" \
  "s_add_u32 s70, s70, 12\n" \
  "s_addc_u32 s71, s71, 0\n" \
  "s_branch Lasub_%=\n" \
  "v_mad_u32_u16 v100, s65, 64, %[c_a]\n" \
  "v_mad_u32_u16 v101, s65, 20, %[c_y]\n" \
  "ds_read_b64_tr_b16 v[216:217], v100\n" \
  "ds_read_b64_tr_b16 v[218:219], v100 offset:128\n" \
  "ds_read_b128 v[200:203], v101\n" \
  "ds_read_b128 v[204:207], v101 offset:32\n" \
  "global_store_dwordx4 v233, v[220:223], s[92:93] sc1\n" \
  "s_add_u32 s92, s92, 0x200\n" \
  "s_addc_u32 s93, s93, 0\n" \
  "v_mfma_f32_32x32x4_2b_f16 v[64:95], v[228:229], v[228:229], 0\n" \
  "v_mfma_f32_32x32x4_2b_f16 v[96:127], v[228:229], v[228:229], 0\n" \
  "s_waitcnt lgkmcnt(0)\n" \
  "v_pk_mul_f16 v200, v200, v216\n" \
  "v_pk_mul_f16 v201, v201, v217\n" \
  "v_pk_mul_f16 v202, v202, v218\n" \
  "v_pk_mul_f16 v203, v203, v219\n" \
  "v_pk_mul_f16 v204, v204, v216\n" \
  "v_pk_mul_f16 v205, v205, v217\n" \
  "v_pk_mul_f16 v206, v206, v218\n" \
  "v_pk_mul_f16 v207, v207, v219\n" \
  "s_setprio 1\n" \
  "s_branch Lret0_%=\n" \
  "Lep10_%=:\n" \
  "s_lshr_b32 s82, s65, 15\n" \
  "s_andn2_b32 s82, s82, 15\n" \
  "v_xad_u32 v240, s82, %[c_x], %[c_u]\n" \
  "s_waitcnt vmcnt(32)\n" \
  "ds_write2_b32 v240, v220, v222 offset1:32\n" \
  "ds_write_b32 v240, v221 offset:1024\n" \
  "ds_write_b32 v240, v223 offset:1152\n" \
  "s_cmp_eq_u32 %[gen], 0\n" \
  "s_cbranch_scc1 Lg10_%=\n" \
  "s_nop 7\n" \
  "ds_read_b128 v[128:131], v232 offset:8704\n" \
  "ds_read_b128 v[132:135], v232 offset:9216\n" \
  "ds_read_b128 v[136:139], v232 offset:9728\n" \
  "ds_read_b128 v[140:143], v232 offset:10240\n" \
  "s_waitcnt lgkmcnt(3)\n" \
  "v_fma_mix_f32 v216, v32, v128, 0 op_sel:[0,0,0] op_sel_hi:[0,1,0]\n" \
  "v_fma_mix_f32 v217, v48, v128, 0 op_sel:[0,1,0] op_sel_hi:[0,1,0]\n" \
  "v_fma_mix_f32 v218, v96, v128, 0 op_sel:[0,0,0] op_sel_hi:[0,1,0]\n" \
  "v_fma_mix_f32 v219, v112, v128, 0 op_sel:[0,1,0] op_sel_hi:[0,1,0]\n" \
  "v_fma_mix_f32 v216, v33, v129, v216 op_sel:[0,0,0] op_sel_hi:[0,1,0]\n" \
  "v_fma_mix_f32 v217, v49, v129, v217 op_sel:[0,1,0] op_sel_hi:[0,1,0]\n" \
  "v_fma_mix_f32 v218, v97, v129, v218 op_sel:[0,0,0] op_sel_hi:[0,1,0]\n" \
  "v_fma_mix_f32 v219, v113, v129, v219 op_sel:[0,1,0] op_sel_hi:[0,1,0]\n" \
  "v_fma_mix_f32 v216, v34, v130, v216 op_sel:[0,0,0] op_sel_hi:[0,1,0]\n" \
  "v_fma_mix_f32 v217, v50, v130, v217 op_sel:[0,1,0] op_sel_hi:[0,1,0]\n" \
  "v_fma_mix_f32 v218, v98, v130, v218 op_sel:[0,0,0] op_sel_hi:[0,1,0]\n" \
  "v_fma_mix_f32 v219, v114, v130, v219 op_sel:[0,1,0] op_sel_hi:[0,1,0]\n" \
  "v_fma_mix_f32 v216, v35, v131, v216 op_sel:[0,0,0] op_sel_hi:[0,1,0]\n" \
  "v_fma_mix_f32 v217, v51, v131, v217 op_sel:[0,1,0] op_sel_hi:[0,1,0]\n" \
  "v_fma_mix_f32 v218, v99, v131, v218 op_sel:[0,0,0] op_sel_hi:[0,1,0]\n" \
  "v_fma_mix_f32 v219, v115, v131, v219 op_sel:[0,1,0] op_sel_hi:[0,1,0]\n" \
  "s_waitcnt lgkmcnt(2)\n" \
  "v_fma_mix_f32 v216, v36, v132, v216 op_sel:[0,0,0] op_sel_hi:[0,1,0]\n" \
  "v_fma_mix_f32 v217, v52, v132, v217 op_sel:[0,1,0] op_sel_hi:[0,1,0]\n" \
  "v_fma_mix_f32 v218, v100, v132, v218 op_sel:[0,0,0] op_sel_hi:[0,1,0]\n" \
  "v_fma_mix_f32 v219, v116, v132, v219 op_sel:[0,1,0] op_sel_hi:[0,1,0]\n" \
  "v_fma_mix_f32 v216, v37, v133, v216 op_sel:[0,0,0] op_sel_hi:[0,1,0]\n" \
  "v_fma_mix_f32 v217, v53, v133, v217 op_sel:[0,1,0] op_sel_hi:[0,1,0]\n" \
  "v_fma_mix_f32 v218, v101, v133, v218 op_sel:[0,0,0] op_sel_hi:[0,1,0]\n" \
  "v_fma_mix_f32 v219, v117, v133, v219 op_sel:[0,1,0] op_sel_hi:[0,1,0]\n" \
  "v_fma_mix_f32 v216, v38, v134, v216 op_sel:[0,0,0] op_sel_hi:[0,1,0]\n" \
  "v_fma_mix_f32 v217, v54, v134, v217 op_sel:[0,1,0] op_sel_hi:[0,1,0]\n" \
  "v_fma_mix_f32 v218, v102, v134, v218 op_sel:[0,0,0] op_sel_hi:[0,1,0]\n" \
  "v_fma_mix_f32 v219, v118, v134, v219 op_sel:[0,1,0] op_sel_hi:[0,1,0]\n" \
  "v_fma_mix_f32 v216, v39, v135, v216 op_sel:[0,0,0] op_sel_hi:[0,1,0]\n" \
  "v_fma_mix_f32 v217, v55, v135, v217 op_sel:[0,1,0] op_sel_hi:[0,1,0]\n" \
  "v_fma_mix_f32 v218, v103, v135, v218 op_sel:[0,0,0] op_sel_hi:[0,1,0]\n" \
  "v_fma_mix_f32 v219, v119, v135, v219 op_sel:[0,1,0] op_sel_hi:[0,1,0]\n" \
  "s_waitcnt lgkmcnt(1)\n" \
  "v_fma_mix_f32 v216, v40, v136, v216 op_sel:[0,0,0] op_sel_hi:[0,1,0]\n" \
  "v_fma_mix_f32 v217, v56, v136, v217 op_sel:[0,1,0] op_sel_hi:[0,1,0]\n" \
  "v_fma_mix_f32 v218, v104, v136, v218 op_sel:[0,0,0] op_sel_hi:[0,1,0]\n" \
  "v_fma_mix_f32 v219, v120, v136, v219 op_sel:[0,1,0] op_sel_hi:[0,1,0]\n" \
  "v_fma_mix_f32 v216, v41, v137, v216 op_sel:[0,0,0] op_sel_hi:[0,1,0]\n" \
  "v_fma_mix_f32 v217, v57, v137, v217 op_sel:[0,1,0] op_sel_hi:[0,1,0]\n" \
  "v_fma_mix_f32 v218, v105, v137, v218 op_sel:[0,0,0] op_sel_hi:[0,1,0]\n" \
  "v_fma_mix_f32 v219, v121, v137, v219 op_sel:[0,1,0] op_sel_hi:[0,1,0]\n" \
  "v_fma_mix_f32 v216, v42, v138, v216 op_sel:[0,0,0] op_sel_hi:[0,1,0]\n" \
  "v_fma_mix_f32 v217, v58, v138, v217 op_sel:[0,1,0] op_sel_hi:[0,1,0]\n" \
  "v_fma_mix_f32 v218, v106, v138, v218 op_sel:[0,0,0] op_sel_hi:[0,1,0]\n" \
  "v_fma_mix_f32 v219, v122, v138, v219 op_sel:[0,1,0] op_sel_hi:[0,1,0]\n" \
  "v_fma_mix_f32 v216, v43, v139, v216 op_sel:[0,0,0] op_sel_hi:[0,1,0]\n" \
  "v_fma_mix_f32 v217, v59, v139, v217 op_sel:[0,1,0] op_sel_hi:[0,1,0]\n" \
  "v_fma_mix_f32 v218, v107, v139, v218 op_sel:[0,0,0] op_sel_hi:[0,1,0]\n" \
  "v_fma_mix_f32 v219, v123, v139, v219 op_sel:[0,1,0] op_sel_hi:[0,1,0]\n" \
  "s_waitcnt lgkmcnt(0)\n" \
  "v_fma_mix_f32 v216, v44, v140, v216 op_sel:[0,0,0] op_sel_hi:[0,1,0]\n" \
  "v_fma_mix_f32 v217, v60, v140, v217 op_sel:[0,1,0] op_sel_hi:[0,1,0]\n" \
  "v_fma_mix_f32 v218, v108, v140, v218 op_sel:[0,0,0] op_sel_hi:[0,1,0]\n" \
  "v_fma_mix_f32 v219, v124, v140, v219 op_sel:[0,1,0] op_sel_hi:[0,1,0]\n" \
  "v_fma_mix_f32 v216, v45, v141, v216 op_sel:[0,0,0] op_sel_hi:[0,1,0]\n" \
  "v_fma_mix_f32 v217, v61, v141, v217 op_sel:[0,1,0] op_sel_hi:[0,1,0]\n" \
  "v_fma_mix_f32 v218, v109, v141, v218 op_sel:[0,0,0] op_sel_hi:[0,1,0]\n" \
  "v_fma_mix_f32 v219, v125, v141, v219 op_sel:[0,1,0] op_sel_hi:[0,1,0]\n" \
  "v_fma_mix_f32 v216, v46, v142, v216 op_sel:[0,0,0] op_sel_hi:[0,1,0]\n" \
  "v_fma_mix_f32 v217, v62, v142, v217 op_sel:[0,1,0] op_sel_hi:[0,1,0]\n" \
  "v_fma_mix_f32 v218, v110, v142, v218 op_sel:[0,0,0] op_sel_hi:[0,1,0]\n" \
  "v_fma_mix_f32 v219, v126, v142, v219 op_sel:[0,1,0] op_sel_hi:[0,1,0]\n" \
  "v_fma_mix_f32 v216, v47, v143, v216 op_sel:[0,0,0] op_sel_hi:[0,1,0]\n" \
  "v_fma_mix_f32 v217, v63, v143, v217 op_sel:[0,1,0] op_sel_hi:[0,1,0]\n" \
  "v_fma_mix_f32 v218, v111, v143, v218 op_sel:[0,0,0] op_sel_hi:[0,1,0]\n" \
  "v_fma_mix_f32 v219, v127, v143, v219 op_sel:[0,1,0] op_sel_hi:[0,1,0]\n" \
  "v_cvt_pk_bf16_f32 v216, v216, v217\n" \
  "v_cvt_pk_bf16_f32 v218, v218, v219\n" \
  "v_cvt_pk_bf16_f32 v217, v230, v231\n" \
  "s_lshr_b32 s83, s65, 24\n" \
  "s_mul_i32 s83, s83, 160\n" \
  "s_and_b32 s82, s82, 0x70\n" \
  "v_xor_b32 v219, s82, %[c_x]\n" \
  "v_add_u32 v219, s83, v219\n" \
  "s_mov_b32 exec_hi, 0\n" \
  "ds_write_b32 v219, v216 offset:%[l_ys]\n" \
  "s_mov_b32 exec_hi, -1\n" \
  "s_mov_b32 exec_lo, 0\n" \
  "ds_write_b32 v219, v217 offset:%[l_ys]+128\n" \
  "ds_write_b32 v219, v218 offset:%[l_ys]+256\n" \
  "s_mov_b32 exec_lo, -1\n" \
  "s_waitcnt lgkmcnt(0)\n" \
  "Lg10_%=:\n" \
  "ds_read_b128 v[128:131], v236 offset:2560\n" \
  "ds_read_b128 v[132:135], v236 offset:3072\n" \
  "ds_read_b128 v[136:139], v236 offset:3584\n" \
  "ds_read_b128 v[140:143], v236 offset:4096\n" \
  "ds_read_b128 v[192:195], v235 offset:6656\n" \
  "ds_read_b128 v[196:199], v235 offset:7168\n" \
  "s_waitcnt lgkmcnt(5)\n" \
  "v_fma_mix_f32 v218, v32, v128, 0 op_sel:[0,0,0] op_sel_hi:[0,1,0]\n" \
  "v_fma_mix_f32 v219, v48, v128, 0 op_sel:[0,1,0] op_sel_hi:[0,1,0]\n" \
  "v_fma_mix_f32 v218, v33, v129, v218 op_sel:[0,0,0] op_sel_hi:[0,1,0]\n" \
  "v_fma_mix_f32 v219, v49, v129, v219 op_sel:[0,1,0] op_sel_hi:[0,1,0]\n" \
  "v_fma_mix_f32 v218, v34, v130, v218 op_sel:[0,0,0] op_sel_hi:[0,1,0]\n" \
  "v_fma_mix_f32 v219, v50, v130, v219 op_sel:[0,1,0] op_sel_hi:[0,1,0]\n" \
  "v_fma_mix_f32 v218, v35, v131, v218 op_sel:[0,0,0] op_sel_hi:[0,1,0]\n" \
  "v_fma_mix_f32 v219, v51, v131, v219 op_sel:[0,1,0] op_sel_hi:[0,1,0]\n" \
  "ds_read_b128 v[128:131], v235 offset:7680\n" \
  "s_waitcnt lgkmcnt(5)\n" \
  "v_fma_mix_f32 v218, v36, v132, v218 op_sel:[0,0,0] op_sel_hi:[0,1,0]\n" \
  "v_fma_mix_f32 v219, v52, v132, v219 op_sel:[0,1,0] op_sel_hi:[0,1,0]\n" \
  "v_fma_mix_f32 v218, v37, v133, v218 op_sel:[0,0,0] op_sel_hi:[0,1,0]\n" \
  "v_fma_mix_f32 v219, v53, v133, v219 op_sel:[0,1,0] op_sel_hi:[0,1,0]\n" \
  "v_fma_mix_f32 v218, v38, v134, v218 op_sel:[0,0,0] op_sel_hi:[0,1,0]\n" \
  "v_fma_mix_f32 v219, v54, v134, v219 op_sel:[0,1,0] op_sel_hi:[0,1,0]\n" \
  "v_fma_mix_f32 v218, v39, v135, v218 op_sel:[0,0,0] op_sel_hi:[0,1,0]\n" \
  "v_fma_mix_f32 v219, v55, v135, v219 op_sel:[0,1,0] op_sel_hi:[0,1,0]\n" \
  "ds_read_b128 v[132:135], v235 offset:8192\n" \
  "s_waitcnt lgkmcnt(5)\n" \
  "v_fma_mix_f32 v218, v40, v136, v218 op_sel:[0,0,0] op_sel_hi:[0,1,0]\n" \
  "v_fma_mix_f32 v219, v56, v136, v219 op_sel:[0,1,0] op_sel_hi:[0,1,0]\n" \
  "v_fma_mix_f32 v218, v41, v137, v218 op_sel:[0,0,0] op_sel_hi:[0,1,0]\n" \
  "v_fma_mix_f32 v219, v57, v137, v219 op_sel:[0,1,0] op_sel_hi:[0,1,0]\n" \
  "v_fma_mix_f32 v218, v42, v138, v218 op_sel:[0,0,0] op_sel_hi:[0,1,0]\n" \
  "v_fma_mix_f32 v219, v58, v138, v219 op_sel:[0,1,0] op_sel_hi:[0,1,0]\n" \
  "v_fma_mix_f32 v218, v43, v139, v218 op_sel:[0,0,0] op_sel_hi:[0,1,0]\n" \
  "v_fma_mix_f32 v219, v59, v139, v219 op_sel:[0,1,0] op_sel_hi:[0,1,0]\n" \
  "ds_read_b128 v[136:139], v237 offset:2560\n" \
  "s_waitcnt lgkmcnt(5)\n" \
  "v_fma_mix_f32 v218, v44, v140, v218 op_sel:[0,0,0] op_sel_hi:[0,1,0]\n" \
  "v_fma_mix_f32 v219, v60, v140, v219 op_sel:[0,1,0] op_sel_hi:[0,1,0]\n" \
  "v_fma_mix_f32 v218, v45, v141, v218 op_sel:[0,0,0] op_sel_hi:[0,1,0]\n" \
  "v_fma_mix_f32 v219, v61, v141, v219 op_sel:[0,1,0] op_sel_hi:[0,1,0]\n" \
  "v_fma_mix_f32 v218, v46, v142, v218 op_sel:[0,0,0] op_sel_hi:[0,1,0]\n" \
  "v_fma_mix_f32 v219, v62, v142, v219 op_sel:[0,1,0] op_sel_hi:[0,1,0]\n" \
  "v_fma_mix_f32 v218, v47, v143, v218 op_sel:[0,0,0] op_sel_hi:[0,1,0]\n" \
  "v_fma_mix_f32 v219, v63, v143, v219 op_sel:[0,1,0] op_sel_hi:[0,1,0]\n" \
  "ds_read_b128 v[140:143], v237 offset:3072\n" \
  "v_mfma_f32_32x32x4_2b_f16 v[32:63], v[228:229], v[228:229], 0\n" \
  "s_waitcnt lgkmcnt(5)\n" \
  "v_fma_mix_f32 v216, v0, v192, 0 op_sel:[0,0,0] op_sel_hi:[0,1,0]\n" \
  "v_fma_mix_f32 v217, v16, v192, 0 op_sel:[0,1,0] op_sel_hi:[0,1,0]\n" \
  "v_fma_mix_f32 v212, v64, v192, 0 op_sel:[0,0,0] op_sel_hi:[0,1,0]\n" \
  "v_fma_mix_f32 v213, v80, v192, 0 op_sel:[0,1,0] op_sel_hi:[0,1,0]\n" \
  "v_fma_mix_f32 v216, v1, v193, v216 op_sel:[0,0,0] op_sel_hi:[0,1,0]\n" \
  "v_fma_mix_f32 v217, v17, v193, v217 op_sel:[0,1,0] op_sel_hi:[0,1,0]\n" \
  "v_fma_mix_f32 v212, v65, v193, v212 op_sel:[0,0,0] op_sel_hi:[0,1,0]\n" \
  "v_fma_mix_f32 v213, v81, v193, v213 op_sel:[0,1,0] op_sel_hi:[0,1,0]\n" \
  "v_fma_mix_f32 v216, v2, v194, v216 op_sel:[0,0,0] op_sel_hi:[0,1,0]\n" \
  "v_fma_mix_f32 v217, v18, v194, v217 op_sel:[0,1,0] op_sel_hi:[0,1,0]\n" \
  "v_fma_mix_f32 v212, v66, v194, v212 op_sel:[0,0,0] op_sel_hi:[0,1,0]\n" \
  "v_fma_mix_f32 v213, v82, v194, v213 op_sel:[0,1,0] op_sel_hi:[0,1,0]\n" \
  "v_fma_mix_f32 v216, v3, v195, v216 op_sel:[0,0,0] op_sel_hi:[0,1,0]\n" \
  "v_fma_mix_f32 v217, v19, v195, v217 op_sel:[0,1,0] op_sel_hi:[0,1,0]\n" \
  "v_fma_mix_f32 v212, v67, v195, v212 op_sel:[0,0,0] op_sel_hi:[0,1,0]\n" \
  "v_fma_mix_f32 v213, v83, v195, v213 op_sel:[0,1,0] op_sel_hi:[0,1,0]\n" \
  "ds_read_b128 v[192:195], v237 offset:3584\n" \
  "s_waitcnt lgkmcnt(5)\n" \
  "v_fma_mix_f32 v216, v4, v196, v216 op_sel:[0,0,0] op_sel_hi:[0,1,0]\n" \
  "v_fma_mix_f32 v217, v20, v196, v217 op_sel:[0,1,0] op_sel_hi:[0,1,0]\n" \
  "v_fma_mix_f32 v212, v68, v196, v212 op_sel:[0,0,0] op_sel_hi:[0,1,0]\n" \
  "v_fma_mix_f32 v213, v84, v196, v213 op_sel:[0,1,0] op_sel_hi:[0,1,0]\n" \
  "v_fma_mix_f32 v216, v5, v197, v216 op_sel:[0,0,0] op_sel_hi:[0,1,0]\n" \
  "v_fma_mix_f32 v217, v21, v197, v217 op_sel:[0,1,0] op_sel_hi:[0,1,0]\n" \
  "v_fma_mix_f32 v212, v69, v197, v212 op_sel:[0,0,0] op_sel_hi:[0,1,0]\n" \
  "v_fma_mix_f32 v213, v85, v197, v213 op_sel:[0,1,0] op_sel_hi:[0,1,0]\n" \
  "v_fma_mix_f32 v216, v6, v198, v216 op_sel:[0,0,0] op_sel_hi:[0,1,0]\n" \
  "v_fma_mix_f32 v217, v22, v198, v217 op_sel:[0,1,0] op_sel_hi:[0,1,0]\n" \
  "v_fma_mix_f32 v212, v70, v198, v212 op_sel:[0,0,0] op_sel_hi:[0,1,0]\n" \
  "v_fma_mix_f32 v213, v86, v198, v213 op_sel:[0,1,0] op_sel_hi:[0,1,0]\n" \
  "v_fma_mix_f32 v216, v7, v199, v216 op_sel:[0,0,0] op_sel_hi:[0,1,0]\n" \
  "v_fma_mix_f32 v217, v23, v199, v217 op_sel:[0,1,0] op_sel_hi:[0,1,0]\n" \
  "v_fma_mix_f32 v212, v71, v199, v212 op_sel:[0,0,0] op_sel_hi:[0,1,0]\n" \
  "v_fma_mix_f32 v213, v87, v199, v213 op_sel:[0,1,0] op_sel_hi:[0,1,0]\n" \
  "ds_read_b128 v[196:199], v237 offset:4096\n" \
  "s_waitcnt lgkmcnt(5)\n" \
  "v_fma_mix_f32 v216, v8, v128, v216 op_sel:[0,0,0] op_sel_hi:[0,1,0]\n" \
  "v_fma_mix_f32 v217, v24, v128, v217 op_sel:[0,1,0] op_sel_hi:[0,1,0]\n" \
  "v_fma_mix_f32 v212, v72, v128, v212 op_sel:[0,0,0] op_sel_hi:[0,1,0]\n" \
  "v_fma_mix_f32 v213, v88, v128, v213 op_sel:[0,1,0] op_sel_hi:[0,1,0]\n" \
  "v_fma_mix_f32 v216, v9, v129, v216 op_sel:[0,0,0] op_sel_hi:[0,1,0]\n" \
  "v_fma_mix_f32 v217, v25, v129, v217 op_sel:[0,1,0] op_sel_hi:[0,1,0]\n" \
  "v_fma_mix_f32 v212, v73, v129, v212 op_sel:[0,0,0] op_sel_hi:[0,1,0]\n" \
  "v_fma_mix_f32 v213, v89, v129, v213 op_sel:[0,1,0] op_sel_hi:[0,1,0]\n" \
  "v_fma_mix_f32 v216, v10, v130, v216 op_sel:[0,0,0] op_sel_hi:[0,1,0]\n" \
  "v_fma_mix_f32 v217, v26, v130, v217 op_sel:[0,1,0] op_sel_hi:[0,1,0]\n" \
  "v_fma_mix_f32 v212, v74, v130, v212 op_sel:[0,0,0] op_sel_hi:[0,1,0]\n" \
  "v_fma_mix_f32 v213, v90, v130, v213 op_sel:[0,1,0] op_sel_hi:[0,1,0]\n" \
  "v_fma_mix_f32 v216, v11, v131, v216 op_sel:[0,0,0] op_sel_hi:[0,1,0]\n" \
  "v_fma_mix_f32 v217, v27, v131, v217 op_sel:[0,1,0] op_sel_hi:[0,1,0]\n" \
  "v_fma_mix_f32 v212, v75, v131, v212 op_sel:[0,0,0] op_sel_hi:[0,1,0]\n" \
  "v_fma_mix_f32 v213, v91, v131, v213 op_sel:[0,1,0] op_sel_hi:[0,1,0]\n" \
  "s_waitcnt lgkmcnt(4)\n" \
  "v_fma_mix_f32 v216, v12, v132, v216 op_sel:[0,0,0] op_sel_hi:[0,1,0]\n" \
  "v_fma_mix_f32 v217, v28, v132, v217 op_sel:[0,1,0] op_sel_hi:[0,1,0]\n" \
  "v_fma_mix_f32 v212, v76, v132, v212 op_sel:[0,0,0] op_sel_hi:[0,1,0]\n" \
  "v_fma_mix_f32 v213, v92, v132, v213 op_sel:[0,1,0] op_sel_hi:[0,1,0]\n" \
  "v_fma_mix_f32 v216, v13, v133, v216 op_sel:[0,0,0] op_sel_hi:[0,1,0]\n" \
  "v_fma_mix_f32 v217, v29, v133, v217 op_sel:[0,1,0] op_sel_hi:[0,1,0]\n" \
  "v_fma_mix_f32 v212, v77, v133, v212 op_sel:[0,0,0] op_sel_hi:[0,1,0]\n" \
  "v_fma_mix_f32 v213, v93, v133, v213 op_sel:[0,1,0] op_sel_hi:[0,1,0]\n" \
  "v_fma_mix_f32 v216, v14, v134, v216 op_sel:[0,0,0] op_sel_hi:[0,1,0]\n" \
  "v_fma_mix_f32 v217, v30, v134, v217 op_sel:[0,1,0] op_sel_hi:[0,1,0]\n" \
  "v_fma_mix_f32 v212, v78, v134, v212 op_sel:[0,0,0] op_sel_hi:[0,1,0]\n" \
  "v_fma_mix_f32 v213, v94, v134, v213 op_sel:[0,1,0] op_sel_hi:[0,1,0]\n" \
  "v_fma_mix_f32 v216, v15, v135, v216 op_sel:[0,0,0] op_sel_hi:[0,1,0]\n" \
  "v_fma_mix_f32 v217, v31, v135, v217 op_sel:[0,1,0] op_sel_hi:[0,1,0]\n" \
  "v_fma_mix_f32 v212, v79, v135, v212 op_sel:[0,0,0] op_sel_hi:[0,1,0]\n" \
  "v_fma_mix_f32 v213, v95, v135, v213 op_sel:[0,1,0] op_sel_hi:[0,1,0]\n" \
  "v_mfma_f32_32x32x4_2b_f16 v[0:31], v[228:229], v[228:229], 0\n" \
  "v_mfma_f32_32x32x4_2b_f16 v[64:95], v[228:229], v[228:229], 0\n" \
  "s_waitcnt lgkmcnt(3)\n" \
  "v_fma_mix_f32 v214, v96, v136, 0 op_sel:[0,0,0] op_sel_hi:[0,1,0]\n" \
  "v_fma_mix_f32 v215, v112, v136, 0 op_sel:[0,1,0] op_sel_hi:[0,1,0]\n" \
  "v_fma_mix_f32 v214, v97, v137, v214 op_sel:[0,0,0] op_sel_hi:[0,1,0]\n" \
  "v_fma_mix_f32 v215, v113, v137, v215 op_sel:[0,1,0] op_sel_hi:[0,1,0]\n" \
  "v_fma_mix_f32 v214, v98, v138, v214 op_sel:[0,0,0] op_sel_hi:[0,1,0]\n" \
  "v_fma_mix_f32 v215, v114, v138, v215 op_sel:[0,1,0] op_sel_hi:[0,1,0]\n" \
  "v_fma_mix_f32 v214, v99, v139, v214 op_sel:[0,0,0] op_sel_hi:[0,1,0]\n" \
  "v_fma_mix_f32 v215, v115, v139, v215 op_sel:[0,1,0] op_sel_hi:[0,1,0]\n" \
  "s_waitcnt lgkmcnt(2)\n" \
  "v_fma_mix_f32 v214, v100, v140, v214 op_sel:[0,0,0] op_sel_hi:[0,1,0]\n" \
  "v_fma_mix_f32 v215, v116, v140, v215 op_sel:[0,1,0] op_sel_hi:[0,1,0]\n" \
  "v_fma_mix_f32 v214, v101, v141, v214 op_sel:[0,0,0] op_sel_hi:[0,1,0]\n" \
  "v_fma_mix_f32 v215, v117, v141, v215 op_sel:[0,1,0] op_sel_hi:[0,1,0]\n" \
  "v_fma_mix_f32 v214, v102, v142, v214 op_sel:[0,0,0] op_sel_hi:[0,1,0]\n" \
  "v_fma_mix_f32 v215, v118, v142, v215 op_sel:[0,1,0] op_sel_hi:[0,1,0]\n" \
  "v_fma_mix_f32 v214, v103, v143, v214 op_sel:[0,0,0] op_sel_hi:[0,1,0]\n" \
  "v_fma_mix_f32 v215, v119, v143, v215 op_sel:[0,1,0] op_sel_hi:[0,1,0]\n" \
  "s_waitcnt lgkmcnt(1)\n" \
  "v_fma_mix_f32 v214, v104, v192, v214 op_sel:[0,0,0] op_sel_hi:[0,1,0]\n" \
  "v_fma_mix_f32 v215, v120, v192, v215 op_sel:[0,1,0] op_sel_hi:[0,1,0]\n" \
  "v_fma_mix_f32 v214, v105, v193, v214 op_sel:[0,0,0] op_sel_hi:[0,1,0]\n" \
  "v_fma_mix_f32 v215, v121, v193, v215 op_sel:[0,1,0] op_sel_hi:[0,1,0]\n" \
  "v_fma_mix_f32 v214, v106, v194, v214 op_sel:[0,0,0] op_sel_hi:[0,1,0]\n" \
  "v_fma_mix_f32 v215, v122, v194, v215 op_sel:[0,1,0] op_sel_hi:[0,1,0]\n" \
  "v_fma_mix_f32 v214, v107, v195, v214 op_sel:[0,0,0] op_sel_hi:[0,1,0]\n" \
  "v_fma_mix_f32 v215, v123, v195, v215 op_sel:[0,1,0] op_sel_hi:[0,1,0]\n" \
  "s_waitcnt lgkmcnt(0)\n" \
  "v_fma_mix_f32 v214, v108, v196, v214 op_sel:[0,0,0] op_sel_hi:[0,1,0]\n" \
  "v_fma_mix_f32 v215, v124, v196, v215 op_sel:[0,1,0] op_sel_hi:[0,1,0]\n" \
  "v_fma_mix_f32 v214, v109, v197, v214 op_sel:[0,0,0] op_sel_hi:[0,1,0]\n" \
  "v_fma_mix_f32 v215, v125, v197, v215 op_sel:[0,1,0] op_sel_hi:[0,1,0]\n" \
  "v_fma_mix_f32 v214, v110, v198, v214 op_sel:[0,0,0] op_sel_hi:[0,1,0]\n" \
  "v_fma_mix_f32 v215, v126, v198, v215 op_sel:[0,1,0] op_sel_hi:[0,1,0]\n" \
  "v_fma_mix_f32 v214, v111, v199, v214 op_sel:[0,0,0] op_sel_hi:[0,1,0]\n" \
  "v_fma_mix_f32 v215, v127, v199, v215 op_sel:[0,1,0] op_sel_hi:[0,1,0]\n" \
  "v_mfma_f32_32x32x4_2b_f16 v[96:127], v[228:229], v[228:229], 0\n" \
  "v_cvt_pk_bf16_f32 v220, v216, v217\n" \
  "v_cvt_pk_bf16_f32 v221, v218, v219\n" \
  "v_cvt_pk_bf16_f32 v222, v212, v213\n" \
  "v_cvt_pk_bf16_f32 v223, v214, v215\n" \
  "ds_write2_b32 v240, v220, v222 offset0:64 offset1:96\n" \
  "ds_write_b32 v240, v221 offset:1280\n" \
  "ds_write_b32 v240, v223 offset:1408\n" \
  "s_sub_u32 s92, s92, 0x200\n" \
  "s_subb_u32 s93, s93, 0\n" \
  "s_bitcmp0_b32 s65, 16\n" \
  "s_cbranch_scc1 Lend_%=\n" \
  "s_setprio 1\n" \
  "s_cmp_eq_u32 %[gen], 0\n" \
  "s_waitcnt lgkmcnt(0)\n" \
  "global_load_dwordx4 v[220:223], v233, s[92:93] offset:-512\n" \
  "s_cbranch_scc1 Lret0_%=\n" \
  "s_add_u32 s94, s92, 0x2000000\n" \
  "s_addc_u32 s95, s93, 0\n" \
  "global_load_dwordx2 v[230:231], v233, s[94:95] offset:-512\n" \
  "s_branch Lret0_%=\n" \
  "Lpc0_%=:\n" \
  "s_mov_b32 m0, s81\n" \
  "s_bitcmp0_b32 s73, 4\n" \
  "s_cselect_b32 exec_hi, -1, 0\n" \
  "global_load_lds_dwordx4 v224, s[74:75]\n" \
  "s_mov_b32 exec_hi, -1\n" \
  "s_add_u32 s81, s81, 0x400\n" \
  "s_lshr_b32 s73, s73, 1\n" \
  "s_mov_b32 s88, s80\n" \
  "v_mov_b32 v224, v225\n" \
  "v_mov_b32 v225, v226\n" \
  "v_mov_b32 v226, v227\n" \
  "s_branch Lpr0_%=\n" \
  "Lwt0_%=:\n" \
  "s_bcnt1_i32_b32 s82, s91\n" \
  "s_cmp_eq_u32 s82, 3\n" \
  "s_waitcnt vmcnt(35)\n" \
  "s_cbranch_scc1 Lwr0_%=\n" \
  "s_cmp_eq_u32 s82, 2\n" \
  "s_waitcnt vmcnt(34)\n" \
  "s_cbranch_scc1 Lwr0_%=\n" \
  "s_waitcnt vmcnt(33)\n" \
  "s_branch Lwr0_%=\n" \
  ".p2align 3\n" \
  "Lepi1_%=:\n" \
  "s_setprio 0\n" \
  "s_bitcmp1_b32 s66, 18\n" \
  "s_cbranch_scc1 Lep11_%=\n" \
  "s_cmp_eq_u32 %[gen], 0\n" \
  "s_cbranch_scc1 Lg01_%=\n" \
  "ds_read_b128 v[144:147], v232 offset:8704\n" \
  "ds_read_b128 v[148:151], v232 offset:9216\n" \
  "ds_read_b128 v[152:155], v232 offset:9728\n" \
  "ds_read_b128 v[156:159], v232 offset:10240\n" \
  "s_waitcnt lgkmcnt(3)\n" \
  "v_fma_mix_f32 v230, v64, v144, 0 op_sel:[0,0,0] op_sel_hi:[0,1,0]\n" \
  "v_fma_mix_f32 v231, v80, v144, 0 op_sel:[0,1,0] op_sel_hi:[0,1,0]\n" \
  "v_fma_mix_f32 v230, v65, v145, v230 op_sel:[0,0,0] op_sel_hi:[0,1,0]\n" \
  "v_fma_mix_f32 v231, v81, v145, v231 op_sel:[0,1,0] op_sel_hi:[0,1,0]\n" \
  "v_fma_mix_f32 v230, v66, v146, v230 op_sel:[0,0,0] op_sel_hi:[0,1,0]\n" \
  "v_fma_mix_f32 v231, v82, v146, v231 op_sel:[0,1,0] op_sel_hi:[0,1,0]\n" \
  "v_fma_mix_f32 v230, v67, v147, v230 op_sel:[0,0,0] op_sel_hi:[0,1,0]\n" \
  "v_fma_mix_f32 v231, v83, v147, v231 op_sel:[0,1,0] op_sel_hi:[0,1,0]\n" \
  "s_waitcnt lgkmcnt(2)\n" \
  "v_fma_mix_f32 v230, v68, v148, v230 op_sel:[0,0,0] op_sel_hi:[0,1,0]\n" \
  "v_fma_mix_f32 v231, v84, v148, v231 op_sel:[0,1,0] op_sel_hi:[0,1,0]\n" \
  "v_fma_mix_f32 v230, v69, v149, v230 op_sel:[0,0,0] op_sel_hi:[0,1,0]\n" \
  "v_fma_mix_f32 v231, v85, v149, v231 op_sel:[0,1,0] op_sel_hi:[0,1,0]\n" \
  "v_fma_mix_f32 v230, v70, v150, v230 op_sel:[0,0,0] op_sel_hi:[0,1,0]\n" \
  "v_fma_mix_f32 v231, v86, v150, v231 op_sel:[0,1,0] op_sel_hi:[0,1,0]\n" \
  "v_fma_mix_f32 v230, v71, v151, v230 op_sel:[0,0,0] op_sel_hi:[0,1,0]\n" \
  "v_fma_mix_f32 v231, v87, v151, v231 op_sel:[0,1,0] op_sel_hi:[0,1,0]\n" \
  "s_waitcnt lgkmcnt(1)\n" \
  "v_fma_mix_f32 v230, v72, v152, v230 op_sel:[0,0,0] op_sel_hi:[0,1,0]\n" \
  "v_fma_mix_f32 v231, v88, v152, v231 op_sel:[0,1,0] op_sel_hi:[0,1,0]\n" \
  "v_fma_mix_f32 v230, v73, v153, v230 op_sel:[0,0,0] op_sel_hi:[0,1,0]\n" \
  "v_fma_mix_f32 v231, v89, v153, v231 op_sel:[0,1,0] op_sel_hi:[0,1,0]\n" \
  "v_fma_mix_f32 v230, v74, v154, v230 op_sel:[0,0,0] op_sel_hi:[0,1,0]\n" \
  "v_fma_mix_f32 v231, v90, v154, v231 op_sel:[0,1,0] op_sel_hi:[0,1,0]\n" \
  "v_fma_mix_f32 v230, v75, v155, v230 op_sel:[0,0,0] op_sel_hi:[0,1,0]\n" \
  "v_fma_mix_f32 v231, v91, v155, v231 op_sel:[0,1,0] op_sel_hi:[0,1,0]\n" \
  "s_waitcnt lgkmcnt(0)\n" \
  "v_fma_mix_f32 v230, v76, v156, v230 op_sel:[0,0,0] op_sel_hi:[0,1,0]\n" \
  "v_fma_mix_f32 v231, v92, v156, v231 op_sel:[0,1,0] op_sel_hi:[0,1,0]\n" \
  "v_fma_mix_f32 v230, v77, v157, v230 op_sel:[0,0,0] op_sel_hi:[0,1,0]\n" \
  "v_fma_mix_f32 v231, v93, v157, v231 op_sel:[0,1,0] op_sel_hi:[0,1,0]\n" \
  "v_fma_mix_f32 v230, v78, v158, v230 op_sel:[0,0,0] op_sel_hi:[0,1,0]\n" \
  "v_fma_mix_f32 v231, v94, v158, v231 op_sel:[0,1,0] op_sel_hi:[0,1,0]\n" \
  "v_fma_mix_f32 v230, v79, v159, v230 op_sel:[0,0,0] op_sel_hi:[0,1,0]\n" \
  "v_fma_mix_f32 v231, v95, v159, v231 op_sel:[0,1,0] op_sel_hi:[0,1,0]\n" \
  "s_add_u32 s94, s92, 0x2000000\n" \
  "s_addc_u32 s95, s93, 0\n" \
  "global_store_dwordx2 v233, v[230:231], s[94:95]\n" \
  "Lg01_%=:\n" \
  "ds_read_b128 v[144:147], v233 offset:512\n" \
  "ds_read_b128 v[148:151], v233 offset:1024\n" \
  "ds_read_b128 v[152:155], v233 offset:1536\n" \
  "ds_read_b128 v[156:159], v233 offset:2048\n" \
  "ds_read_b128 v[200:203], v232 offset:4608\n" \
  "ds_read_b128 v[204:207], v232 offset:5120\n" \
  "s_waitcnt lgkmcnt(5)\n" \
  "v_fma_mix_f32 v216, v0, v144, 0 op_sel:[0,0,0] op_sel_hi:[0,1,0]\n" \
  "v_fma_mix_f32 v217, v16, v144, 0 op_sel:[0,1,0] op_sel_hi:[0,1,0]\n" \
  "v_fma_mix_f32 v216, v1, v145, v216 op_sel:[0,0,0] op_sel_hi:[0,1,0]\n" \
  "v_fma_mix_f32 v217, v17, v145, v217 op_sel:[0,1,0] op_sel_hi:[0,1,0]\n" \
  "v_fma_mix_f32 v216, v2, v146, v216 op_sel:[0,0,0] op_sel_hi:[0,1,0]\n" \
  "v_fma_mix_f32 v217, v18, v146, v217 op_sel:[0,1,0] op_sel_hi:[0,1,0]\n" \
  "v_fma_mix_f32 v216, v3, v147, v216 op_sel:[0,0,0] op_sel_hi:[0,1,0]\n" \
  "v_fma_mix_f32 v217, v19, v147, v217 op_sel:[0,1,0] op_sel_hi:[0,1,0]\n" \
  "ds_read_b128 v[144:147], v232 offset:5632\n" \
  "s_waitcnt lgkmcnt(5)\n" \
  "v_fma_mix_f32 v216, v4, v148, v216 op_sel:[0,0,0] op_sel_hi:[0,1,0]\n" \
  "v_fma_mix_f32 v217, v20, v148, v217 op_sel:[0,1,0] op_sel_hi:[0,1,0]\n" \
  "v_fma_mix_f32 v216, v5, v149, v216 op_sel:[0,0,0] op_sel_hi:[0,1,0]\n" \
  "v_fma_mix_f32 v217, v21, v149, v217 op_sel:[0,1,0] op_sel_hi:[0,1,0]\n" \
  "v_fma_mix_f32 v216, v6, v150, v216 op_sel:[0,0,0] op_sel_hi:[0,1,0]\n" \
  "v_fma_mix_f32 v217, v22, v150, v217 op_sel:[0,1,0] op_sel_hi:[0,1,0]\n" \
  "v_fma_mix_f32 v216, v7, v151, v216 op_sel:[0,0,0] op_sel_hi:[0,1,0]\n" \
  "v_fma_mix_f32 v217, v23, v151, v217 op_sel:[0,1,0] op_sel_hi:[0,1,0]\n" \
  "ds_read_b128 v[148:151], v232 offset:6144\n" \
  "s_waitcnt lgkmcnt(5)\n" \
  "v_fma_mix_f32 v216, v8, v152, v216 op_sel:[0,0,0] op_sel_hi:[0,1,0]\n" \
  "v_fma_mix_f32 v217, v24, v152, v217 op_sel:[0,1,0] op_sel_hi:[0,1,0]\n" \
  "v_fma_mix_f32 v216, v9, v153, v216 op_sel:[0,0,0] op_sel_hi:[0,1,0]\n" \
  "v_fma_mix_f32 v217, v25, v153, v217 op_sel:[0,1,0] op_sel_hi:[0,1,0]\n" \
  "v_fma_mix_f32 v216, v10, v154, v216 op_sel:[0,0,0] op_sel_hi:[0,1,0]\n" \
  "v_fma_mix_f32 v217, v26, v154, v217 op_sel:[0,1,0] op_sel_hi:[0,1,0]\n" \
  "v_fma_mix_f32 v216, v11, v155, v216 op_sel:[0,0,0] op_sel_hi:[0,1,0]\n" \
  "v_fma_mix_f32 v217, v27, v155, v217 op_sel:[0,1,0] op_sel_hi:[0,1,0]\n" \
  "ds_read_b128 v[152:155], v234 offset:2560\n" \
  "s_waitcnt lgkmcnt(5)\n" \
  "v_fma_mix_f32 v216, v12, v156, v216 op_sel:[0,0,0] op_sel_hi:[0,1,0]\n" \
  "v_fma_mix_f32 v217, v28, v156, v217 op_sel:[0,1,0] op_sel_hi:[0,1,0]\n" \
  "v_fma_mix_f32 v216, v13, v157, v216 op_sel:[0,0,0] op_sel_hi:[0,1,0]\n" \
  "v_fma_mix_f32 v217, v29, v157, v217 op_sel:[0,1,0] op_sel_hi:[0,1,0]\n" \
  "v_fma_mix_f32 v216, v14, v158, v216 op_sel:[0,0,0] op_sel_hi:[0,1,0]\n" \
  "v_fma_mix_f32 v217, v30, v158, v217 op_sel:[0,1,0] op_sel_hi:[0,1,0]\n" \
  "v_fma_mix_f32 v216, v15, v159, v216 op_sel:[0,0,0] op_sel_hi:[0,1,0]\n" \
  "v_fma_mix_f32 v217, v31, v159, v217 op_sel:[0,1,0] op_sel_hi:[0,1,0]\n" \
  "ds_read_b128 v[156:159], v234 offset:3072\n" \
  "v_mfma_f32_32x32x4_2b_f16 v[0:31], v[228:229], v[228:229], 0\n" \
  "s_waitcnt lgkmcnt(5)\n" \
  "v_fma_mix_f32 v218, v32, v200, 0 op_sel:[0,0,0] op_sel_hi:[0,1,0]\n" \
  "v_fma_mix_f32 v219, v48, v200, 0 op_sel:[0,1,0] op_sel_hi:[0,1,0]\n" \
  "v_fma_mix_f32 v218, v33, v201, v218 op_sel:[0,0,0] op_sel_hi:[0,1,0]\n" \
  "v_fma_mix_f32 v219, v49, v201, v219 op_sel:[0,1,0] op_sel_hi:[0,1,0]\n" \
  "v_fma_mix_f32 v218, v34, v202, v218 op_sel:[0,0,0] op_sel_hi:[0,1,0]\n" \
  "v_fma_mix_f32 v219, v50, v202, v219 op_sel:[0,1,0] op_sel_hi:[0,1,0]\n" \
  "v_fma_mix_f32 v218, v35, v203, v218 op_sel:[0,0,0] op_sel_hi:[0,1,0]\n" \
  "v_fma_mix_f32 v219, v51, v203, v219 op_sel:[0,1,0] op_sel_hi:[0,1,0]\n" \
  "ds_read_b128 v[200:203], v234 offset:3584\n" \
  "s_waitcnt lgkmcnt(5)\n" \
  "v_fma_mix_f32 v218, v36, v204, v218 op_sel:[0,0,0] op_sel_hi:[0,1,0]\n" \
  "v_fma_mix_f32 v219, v52, v204, v219 op_sel:[0,1,0] op_sel_hi:[0,1,0]\n" \
  "v_fma_mix_f32 v218, v37, v205, v218 op_sel:[0,0,0] op_sel_hi:[0,1,0]\n" \
  "v_fma_mix_f32 v219, v53, v205, v219 op_sel:[0,1,0] op_sel_hi:[0,1,0]\n" \
  "v_fma_mix_f32 v218, v38, v206, v218 op_sel:[0,0,0] op_sel_hi:[0,1,0]\n" \
  "v_fma_mix_f32 v219, v54, v206, v219 op_sel:[0,1,0] op_sel_hi:[0,1,0]\n" \
  "v_fma_mix_f32 v218, v39, v207, v218 op_sel:[0,0,0] op_sel_hi:[0,1,0]\n" \
  "v_fma_mix_f32 v219, v55, v207, v219 op_sel:[0,1,0] op_sel_hi:[0,1,0]\n" \
  "ds_read_b128 v[204:207], v234 offset:4096\n" \
  "s_waitcnt lgkmcnt(5)\n" \
  "v_fma_mix_f32 v218, v40, v144, v218 op_sel:[0,0,0] op_sel_hi:[0,1,0]\n" \
  "v_fma_mix_f32 v219, v56, v144, v219 op_sel:[0,1,0] op_sel_hi:[0,1,0]\n" \
  "v_fma_mix_f32 v218, v41, v145, v218 op_sel:[0,0,0] op_sel_hi:[0,1,0]\n" \
  "v_fma_mix_f32 v219, v57, v145, v219 op_sel:[0,1,0] op_sel_hi:[0,1,0]\n" \
  "v_fma_mix_f32 v218, v42, v146, v218 op_sel:[0,0,0] op_sel_hi:[0,1,0]\n" \
  "v_fma_mix_f32 v219, v58, v146, v219 op_sel:[0,1,0] op_sel_hi:[0,1,0]\n" \
  "v_fma_mix_f32 v218, v43, v147, v218 op_sel:[0,0,0] op_sel_hi:[0,1,0]\n" \
  "v_fma_mix_f32 v219, v59, v147, v219 op_sel:[0,1,0] op_sel_hi:[0,1,0]\n" \
  "ds_read_b128 v[144:147], v232 offset:8704\n" \
  "s_waitcnt lgkmcnt(5)\n" \
  "v_fma_mix_f32 v218, v44, v148, v218 op_sel:[0,0,0] op_sel_hi:[0,1,0]\n" \
  "v_fma_mix_f32 v219, v60, v148, v219 op_sel:[0,1,0] op_sel_hi:[0,1,0]\n" \
  "v_fma_mix_f32 v218, v45, v149, v218 op_sel:[0,0,0] op_sel_hi:[0,1,0]\n" \
  "v_fma_mix_f32 v219, v61, v149, v219 op_sel:[0,1,0] op_sel_hi:[0,1,0]\n" \
  "v_fma_mix_f32 v218, v46, v150, v218 op_sel:[0,0,0] op_sel_hi:[0,1,0]\n" \
  "v_fma_mix_f32 v219, v62, v150, v219 op_sel:[0,1,0] op_sel_hi:[0,1,0]\n" \
  "v_fma_mix_f32 v218, v47, v151, v218 op_sel:[0,0,0] op_sel_hi:[0,1,0]\n" \
  "v_fma_mix_f32 v219, v63, v151, v219 op_sel:[0,1,0] op_sel_hi:[0,1,0]\n" \
  "ds_read_b128 v[148:151], v232 offset:9216\n" \
  "v_mfma_f32_32x32x4_2b_f16 v[32:63], v[228:229], v[228:229], 0\n" \
  "s_waitcnt lgkmcnt(5)\n" \
  "v_fma_mix_f32 v208, v64, v152, 0 op_sel:[0,0,0] op_sel_hi:[0,1,0]\n" \
  "v_fma_mix_f32 v209, v80, v152, 0 op_sel:[0,1,0] op_sel_hi:[0,1,0]\n" \
  "v_fma_mix_f32 v208, v65, v153, v208 op_sel:[0,0,0] op_sel_hi:[0,1,0]\n" \
  "v_fma_mix_f32 v209, v81, v153, v209 op_sel:[0,1,0] op_sel_hi:[0,1,0]\n" \
  "v_fma_mix_f32 v208, v66, v154, v208 op_sel:[0,0,0] op_sel_hi:[0,1,0]\n" \
  "v_fma_mix_f32 v209, v82, v154, v209 op_sel:[0,1,0] op_sel_hi:[0,1,0]\n" \
  "v_fma_mix_f32 v208, v67, v155, v208 op_sel:[0,0,0] op_sel_hi:[0,1,0]\n" \
  "v_fma_mix_f32 v209, v83, v155, v209 op_sel:[0,1,0] op_sel_hi:[0,1,0]\n" \
  "ds_read_b128 v[152:155], v232 offset:9728\n" \
  "s_waitcnt lgkmcnt(5)\n" \
  "v_fma_mix_f32 v208, v68, v156, v208 op_sel:[0,0,0] op_sel_hi:[0,1,0]\n" \
  "v_fma_mix_f32 v209, v84, v156, v209 op_sel:[0,1,0] op_sel_hi:[0,1,0]\n" \
  "v_fma_mix_f32 v208, v69, v157, v208 op_sel:[0,0,0] op_sel_hi:[0,1,0]\n" \
  "v_fma_mix_f32 v209, v85, v157, v209 op_sel:[0,1,0] op_sel_hi:[0,1,0]\n" \
  "v_fma_mix_f32 v208, v70, v158, v208 op_sel:[0,0,0] op_sel_hi:[0,1,0]\n" \
  "v_fma_mix_f32 v209, v86, v158, v209 op_sel:[0,1,0] op_sel_hi:[0,1,0]\n" \
  "v_fma_mix_f32 v208, v71, v159, v208 op_sel:[0,0,0] op_sel_hi:[0,1,0]\n" \
  "v_fma_mix_f32 v209, v87, v159, v209 op_sel:[0,1,0] op_sel_hi:[0,1,0]\n" \
  "ds_read_b128 v[156:159], v232 offset:10240\n" \
  "s_waitcnt lgkmcnt(5)\n" \
  "v_fma_mix_f32 v208, v72, v200, v208 op_sel:[0,0,0] op_sel_hi:[0,1,0]\n" \
  "v_fma_mix_f32 v209, v88, v200, v209 op_sel:[0,1,0] op_sel_hi:[0,1,0]\n" \
  "v_fma_mix_f32 v208, v73, v201, v208 op_sel:[0,0,0] op_sel_hi:[0,1,0]\n" \
  "v_fma_mix_f32 v209, v89, v201, v209 op_sel:[0,1,0] op_sel_hi:[0,1,0]\n" \
  "v_fma_mix_f32 v208, v74, v202, v208 op_sel:[0,0,0] op_sel_hi:[0,1,0]\n" \
  "v_fma_mix_f32 v209, v90, v202, v209 op_sel:[0,1,0] op_sel_hi:[0,1,0]\n" \
  "v_fma_mix_f32 v208, v75, v203, v208 op_sel:[0,0,0] op_sel_hi:[0,1,0]\n" \
  "v_fma_mix_f32 v209, v91, v203, v209 op_sel:[0,1,0] op_sel_hi:[0,1,0]\n" \
  "s_waitcnt lgkmcnt(4)\n" \
  "v_fma_mix_f32 v208, v76, v204, v208 op_sel:[0,0,0] op_sel_hi:[0,1,0]\n" \
  "v_fma_mix_f32 v209, v92, v204, v209 op_sel:[0,1,0] op_sel_hi:[0,1,0]\n" \
  "v_fma_mix_f32 v208, v77, v205, v208 op_sel:[0,0,0] op_sel_hi:[0,1,0]\n" \
  "v_fma_mix_f32 v209, v93, v205, v209 op_sel:[0,1,0] op_sel_hi:[0,1,0]\n" \
  "v_fma_mix_f32 v208, v78, v206, v208 op_sel:[0,0,0] op_sel_hi:[0,1,0]\n" \
  "v_fma_mix_f32 v209, v94, v206, v209 op_sel:[0,1,0] op_sel_hi:[0,1,0]\n" \
  "v_fma_mix_f32 v208, v79, v207, v208 op_sel:[0,0,0] op_sel_hi:[0,1,0]\n" \
  "v_fma_mix_f32 v209, v95, v207, v209 op_sel:[0,1,0] op_sel_hi:[0,1,0]\n" \
  "s_waitcnt lgkmcnt(3)\n" \
  "v_fma_mix_f32 v210, v96, v144, 0 op_sel:[0,0,0] op_sel_hi:[0,1,0]\n" \
  "v_fma_mix_f32 v211, v112, v144, 0 op_sel:[0,1,0] op_sel_hi:[0,1,0]\n" \
  "v_fma_mix_f32 v210, v97, v145, v210 op_sel:[0,0,0] op_sel_hi:[0,1,0]\n" \
  "v_fma_mix_f32 v211, v113, v145, v211 op_sel:[0,1,0] op_sel_hi:[0,1,0]\n" \
  "v_fma_mix_f32 v210, v98, v146, v210 op_sel:[0,0,0] op_sel_hi:[0,1,0]\n" \
  "v_fma_mix_f32 v211, v114, v146, v211 op_sel:[0,1,0] op_sel_hi:[0,1,0]\n" \
  "v_fma_mix_f32 v210, v99, v147, v210 op_sel:[0,0,0] op_sel_hi:[0,1,0]\n" \
  "v_fma_mix_f32 v211, v115, v147, v211 op_sel:[0,1,0] op_sel_hi:[0,1,0]\n" \
  "s_waitcnt lgkmcnt(2)\n" \
  "v_fma_mix_f32 v210, v100, v148, v210 op_sel:[0,0,0] op_sel_hi:[0,1,0]\n" \
  "v_fma_mix_f32 v211, v116, v148, v211 op_sel:[0,1,0] op_sel_hi:[0,1,0]\n" \
  "v_fma_mix_f32 v210, v101, v149, v210 op_sel:[0,0,0] op_sel_hi:[0,1,0]\n" \
  "v_fma_mix_f32 v211, v117, v149, v211 op_sel:[0,1,0] op_sel_hi:[0,1,0]\n" \
  "v_fma_mix_f32 v210, v102, v150, v210 op_sel:[0,0,0] op_sel_hi:[0,1,0]\n" \
  "v_fma_mix_f32 v211, v118, v150, v211 op_sel:[0,1,0] op_sel_hi:[0,1,0]\n" \
  "v_fma_mix_f32 v210, v103, v151, v210 op_sel:[0,0,0] op_sel_hi:[0,1,0]\n" \
  "v_fma_mix_f32 v211, v119, v151, v211 op_sel:[0,1,0] op_sel_hi:[0,1,0]\n" \
  "s_waitcnt lgkmcnt(1)\n" \
  "v_fma_mix_f32 v210, v104, v152, v210 op_sel:[0,0,0] op_sel_hi:[0,1,0]\n" \
  "v_fma_mix_f32 v211, v120, v152, v211 op_sel:[0,1,0] op_sel_hi:[0,1,0]\n" \
  "v_fma_mix_f32 v210, v105, v153, v210 op_sel:[0,0,0] op_sel_hi:[0,1,0]\n" \
  "v_fma_mix_f32 v211, v121, v153, v211 op_sel:[0,1,0] op_sel_hi:[0,1,0]\n" \
  "v_fma_mix_f32 v210, v106, v154, v210 op_sel:[0,0,0] op_sel_hi:[0,1,0]\n" \
  "v_fma_mix_f32 v211, v122, v154, v211 op_sel:[0,1,0] op_sel_hi:[0,1,0]\n" \
  "v_fma_mix_f32 v210, v107, v155, v210 op_sel:[0,0,0] op_sel_hi:[0,1,0]\n" \
  "v_fma_mix_f32 v211, v123, v155, v211 op_sel:[0,1,0] op_sel_hi:[0,1,0]\n" \
  "s_waitcnt lgkmcnt(0)\n" \
  "v_fma_mix_f32 v210, v108, v156, v210 op_sel:[0,0,0] op_sel_hi:[0,1,0]\n" \
  "v_fma_mix_f32 v211, v124, v156, v211 op_sel:[0,1,0] op_sel_hi:[0,1,0]\n" \
  "v_fma_mix_f32 v210, v109, v157, v210 op_sel:[0,0,0] op_sel_hi:[0,1,0]\n" \
  "v_fma_mix_f32 v211, v125, v157, v211 op_sel:[0,1,0] op_sel_hi:[0,1,0]\n" \
  "v_fma_mix_f32 v210, v110, v158, v210 op_sel:[0,0,0] op_sel_hi:[0,1,0]\n" \
  "v_fma_mix_f32 v211, v126, v158, v211 op_sel:[0,1,0] op_sel_hi:[0,1,0]\n" \
  "v_fma_mix_f32 v210, v111, v159, v210 op_sel:[0,0,0] op_sel_hi:[0,1,0]\n" \
  "v_fma_mix_f32 v211, v127, v159, v211 op_sel:[0,1,0] op_sel_hi:[0,1,0]\n" \
  "v_cvt_pk_bf16_f32 v220, v216, v217\n" \
  "v_cvt_pk_bf16_f32 v221, v218, v219\n" \
  "v_cvt_pk_bf16_f32 v222, v208, v209\n" \
  "v_cvt_pk_bf16_f32 v223, v210, v211\n" \
  "s_getpc_b64 s[70:71]\n" \
  "s_add_u32 s70, s70, 12\n" \
  "s_addc_u32 s71, s71, 0\n" \
  "s_branch Lasub_%=\n" \
  "v_mad_u32_u16 v100, s66, 64, %[c_a]\n" \
  "v_mad_u32_u16 v101, s66, 20, %[c_y]\n" \
  "ds_read_b64_tr_b16 v[216:217], v100\n" \
  "ds_read_b64_tr_b16 v[218:219], v100 offset:128\n" \
  "ds_read_b128 v[192:195], v101\n" \
  "ds_read_b128 v[196:199], v101 offset:32\n" \
  "global_store_dwordx4 v233, v[220:223], s[92:93] sc1\n" \
  "s_add_u32 s92, s92, 0x200\n" \
  "s_addc_u32 s93, s93, 0\n" \
  "v_mfma_f32_32x32x4_2b_f16 v[64:95], v[228:229], v[228:229], 0\n" \
  "v_mfma_f32_32x32x4_2b_f16 v[96:127], v[228:229], v[228:229], 0\n" \
  "s_waitcnt lgkmcnt(0)\n" \
  "v_pk_mul_f16 v192, v192, v216\n" \
  "v_pk_mul_f16 v193, v193, v217\n" \
  "v_pk_mul_f16 v194, v194, v218\n" \
  "v_pk_mul_f16 v195, v195, v219\n" \
  "v_pk_mul_f16 v196, v196, v216\n" \
  "v_pk_mul_f16 v197, v197, v217\n" \
  "v_pk_mul_f16 v198, v198, v218\n" \
  "v_pk_mul_f16 v199, v199, v219\n" \
  "s_setprio 1\n" \
  "s_branch Lret1_%=\n" \
  "Lep11_%=:\n" \
  "s_lshr_b32 s82, s66, 15\n" \
  "s_andn2_b32 s82, s82, 15\n" \
  "v_xad_u32 v240, s82, %[c_x], %[c_u]\n" \
  "s_waitcnt vmcnt(32)\n" \
  "ds_write2_b32 v240, v220, v222 offset1:32\n" \
  "ds_write_b32 v240, v221 offset:1024\n" \
  "ds_write_b32 v240, v223 offset:1152\n" \
  "s_cmp_eq_u32 %[gen], 0\n" \
  "s_cbranch_scc1 Lg11_%=\n" \
  "s_nop 7\n" \
  "ds_read_b128 v[144:147], v232 offset:8704\n" \
  "ds_read_b128 v[148:151], v232 offset:9216\n" \
  "ds_read_b128 v[152:155], v232 offset:9728\n" \
  "ds_read_b128 v[156:159], v232 offset:10240\n" \
  "s_waitcnt lgkmcnt(3)\n" \
  "v_fma_mix_f32 v216, v32, v144, 0 op_sel:[0,0,0] op_sel_hi:[0,1,0]\n" \
  "v_fma_mix_f32 v217, v48, v144, 0 op_sel:[0,1,0] op_sel_hi:[0,1,0]\n" \
  "v_fma_mix_f32 v218, v96, v144, 0 op_sel:[0,0,0] op_sel_hi:[0,1,0]\n" \
  "v_fma_mix_f32 v219, v112, v144, 0 op_sel:[0,1,0] op_sel_hi:[0,1,0]\n" \
  "v_fma_mix_f32 v216, v33, v145, v216 op_sel:[0,0,0] op_sel_hi:[0,1,0]\n" \
  "v_fma_mix_f32 v217, v49, v145, v217 op_sel:[0,1,0] op_sel_hi:[0,1,0]\n" \
  "v_fma_mix_f32 v218, v97, v145, v218 op_sel:[0,0,0] op_sel_hi:[0,1,0]\n" \
  "v_fma_mix_f32 v219, v113, v145, v219 op_sel:[0,1,0] op_sel_hi:[0,1,0]\n" \
  "v_fma_mix_f32 v216, v34, v146, v216 op_sel:[0,0,0] op_sel_hi:[0,1,0]\n" \
  "v_fma_mix_f32 v217, v50, v146, v217 op_sel:[0,1,0] op_sel_hi:[0,1,0]\n" \
  "v_fma_mix_f32 v218, v98, v146, v218 op_sel:[0,0,0] op_sel_hi:[0,1,0]\n" \
  "v_fma_mix_f32 v219, v114, v146, v219 op_sel:[0,1,0] op_sel_hi:[0,1,0]\n" \
  "v_fma_mix_f32 v216, v35, v147, v216 op_sel:[0,0,0] op_sel_hi:[0,1,0]\n" \
  "v_fma_mix_f32 v217, v51, v147, v217 op_sel:[0,1,0] op_sel_hi:[0,1,0]\n" \
  "v_fma_mix_f32 v218, v99, v147, v218 op_sel:[0,0,0] op_sel_hi:[0,1,0]\n" \
  "v_fma_mix_f32 v219, v115, v147, v219 op_sel:[0,1,0] op_sel_hi:[0,1,0]\n" \
  "s_waitcnt lgkmcnt(2)\n" \
  "v_fma_mix_f32 v216, v36, v148, v216 op_sel:[0,0,0] op_sel_hi:[0,1,0]\n" \
  "v_fma_mix_f32 v217, v52, v148, v217 op_sel:[0,1,0] op_sel_hi:[0,1,0]\n" \
  "v_fma_mix_f32 v218, v100, v148, v218 op_sel:[0,0,0] op_sel_hi:[0,1,0]\n" \
  "v_fma_mix_f32 v219, v116, v148, v219 op_sel:[0,1,0] op_sel_hi:[0,1,0]\n" \
  "v_fma_mix_f32 v216, v37, v149, v216 op_sel:[0,0,0] op_sel_hi:[0,1,0]\n" \
  "v_fma_mix_f32 v217, v53, v149, v217 op_sel:[0,1,0] op_sel_hi:[0,1,0]\n" \
  "v_fma_mix_f32 v218, v101, v149, v218 op_sel:[0,0,0] op_sel_hi:[0,1,0]\n" \
  "v_fma_mix_f32 v219, v117, v149, v219 op_sel:[0,1,0] op_sel_hi:[0,1,0]\n" \
  "v_fma_mix_f32 v216, v38, v150, v216 op_sel:[0,0,0] op_sel_hi:[0,1,0]\n" \
  "v_fma_mix_f32 v217, v54, v150, v217 op_sel:[0,1,0] op_sel_hi:[0,1,0]\n" \
  "v_fma_mix_f32 v218, v102, v150, v218 op_sel:[0,0,0] op_sel_hi:[0,1,0]\n" \
  "v_fma_mix_f32 v219, v118, v150, v219 op_sel:[0,1,0] op_sel_hi:[0,1,0]\n" \
  "v_fma_mix_f32 v216, v39, v151, v216 op_sel:[0,0,0] op_sel_hi:[0,1,0]\n" \
  "v_fma_mix_f32 v217, v55, v151, v217 op_sel:[0,1,0] op_sel_hi:[0,1,0]\n" \
  "v_fma_mix_f32 v218, v103, v151, v218 op_sel:[0,0,0] op_sel_hi:[0,1,0]\n" \
  "v_fma_mix_f32 v219, v119, v151, v219 op_sel:[0,1,0] op_sel_hi:[0,1,0]\n" \
  "s_waitcnt lgkmcnt(1)\n" \
  "v_fma_mix_f32 v216, v40, v152, v216 op_sel:[0,0,0] op_sel_hi:[0,1,0]\n" \
  "v_fma_mix_f32 v217, v56, v152, v217 op_sel:[0,1,0] op_sel_hi:[0,1,0]\n" \
  "v_fma_mix_f32 v218, v104, v152, v218 op_sel:[0,0,0] op_sel_hi:[0,1,0]\n" \
  "v_fma_mix_f32 v219, v120, v152, v219 op_sel:[0,1,0] op_sel_hi:[0,1,0]\n" \
  "v_fma_mix_f32 v216, v41, v153, v216 op_sel:[0,0,0] op_sel_hi:[0,1,0]\n" \
  "v_fma_mix_f32 v217, v57, v153, v217 op_sel:[0,1,0] op_sel_hi:[0,1,0]\n" \
  "v_fma_mix_f32 v218, v105, v153, v218 op_sel:[0,0,0] op_sel_hi:[0,1,0]\n" \
  "v_fma_mix_f32 v219, v121, v153, v219 op_sel:[0,1,0] op_sel_hi:[0,1,0]\n" \
  "v_fma_mix_f32 v216, v42, v154, v216 op_sel:[0,0,0] op_sel_hi:[0,1,0]\n" \
  "v_fma_mix_f32 v217, v58, v154, v217 op_sel:[0,1,0] op_sel_hi:[0,1,0]\n" \
  "v_fma_mix_f32 v218, v106, v154, v218 op_sel:[0,0,0] op_sel_hi:[0,1,0]\n" \
  "v_fma_mix_f32 v219, v122, v154, v219 op_sel:[0,1,0] op_sel_hi:[0,1,0]\n" \
  "v_fma_mix_f32 v216, v43, v155, v216 op_sel:[0,0,0] op_sel_hi:[0,1,0]\n" \
  "v_fma_mix_f32 v217, v59, v155, v217 op_sel:[0,1,0] op_sel_hi:[0,1,0]\n" \
  "v_fma_mix_f32 v218, v107, v155, v218 op_sel:[0,0,0] op_sel_hi:[0,1,0]\n" \
  "v_fma_mix_f32 v219, v123, v155, v219 op_sel:[0,1,0] op_sel_hi:[0,1,0]\n" \
  "s_waitcnt lgkmcnt(0)\n" \
  "v_fma_mix_f32 v216, v44, v156, v216 op_sel:[0,0,0] op_sel_hi:[0,1,0]\n" \
  "v_fma_mix_f32 v217, v60, v156, v217 op_sel:[0,1,0] op_sel_hi:[0,1,0]\n" \
  "v_fma_mix_f32 v218, v108, v156, v218 op_sel:[0,0,0] op_sel_hi:[0,1,0]\n" \
  "v_fma_mix_f32 v219, v124, v156, v219 op_sel:[0,1,0] op_sel_hi:[0,1,0]\n" \
  "v_fma_mix_f32 v216, v45, v157, v216 op_sel:[0,0,0] op_sel_hi:[0,1,0]\n" \
  "v_fma_mix_f32 v217, v61, v157, v217 op_sel:[0,1,0] op_sel_hi:[0,1,0]\n" \
  "v_fma_mix_f32 v218, v109, v157, v218 op_sel:[0,0,0] op_sel_hi:[0,1,0]\n" \
  "v_fma_mix_f32 v219, v125, v157, v219 op_sel:[0,1,0] op_sel_hi:[0,1,0]\n" \
  "v_fma_mix_f32 v216, v46, v158, v216 op_sel:[0,0,0] op_sel_hi:[0,1,0]\n" \
  "v_fma_mix_f32 v217, v62, v158, v217 op_sel:[0,1,0] op_sel_hi:[0,1,0]\n" \
  "v_fma_mix_f32 v218, v110, v158, v218 op_sel:[0,0,0] op_sel_hi:[0,1,0]\n" \
  "v_fma_mix_f32 v219, v126, v158, v219 op_sel:[0,1,0] op_sel_hi:[0,1,0]\n" \
  "v_fma_mix_f32 v216, v47, v159, v216 op_sel:[0,0,0] op_sel_hi:[0,1,0]\n" \
  "v_fma_mix_f32 v217, v63, v159, v217 op_sel:[0,1,0] op_sel_hi:[0,1,0]\n" \
  "v_fma_mix_f32 v218, v111, v159, v218 op_sel:[0,0,0] op_sel_hi:[0,1,0]\n" \
  "v_fma_mix_f32 v219, v127, v159, v219 op_sel:[0,1,0] op_sel_hi:[0,1,0]\n" \
  "v_cvt_pk_bf16_f32 v216, v216, v217\n" \
  "v_cvt_pk_bf16_f32 v218, v218, v219\n" \
  "v_cvt_pk_bf16_f32 v217, v230, v231\n" \
  "s_lshr_b32 s83, s66, 24\n" \
  "s_mul_i32 s83, s83, 160\n" \
  "s_and_b32 s82, s82, 0x70\n" \
  "v_xor_b32 v219, s82, %[c_x]\n" \
  "v_add_u32 v219, s83, v219\n" \
  "s_mov_b32 exec_hi, 0\n" \
  "ds_write_b32 v219, v216 offset:%[l_ys]\n" \
  "s_mov_b32 exec_hi, -1\n" \
  "s_mov_b32 exec_lo, 0\n" \
  "ds_write_b32 v219, v217 offset:%[l_ys]+128\n" \
  "ds_write_b32 v219, v218 offset:%[l_ys]+256\n" \
  "s_mov_b32 exec_lo, -1\n" \
  "s_waitcnt lgkmcnt(0)\n" \
  "Lg11_%=:\n" \
  "ds_read_b128 v[144:147], v236 offset:2560\n" \
  "ds_read_b128 v[148:151], v236 offset:3072\n" \
  "ds_read_b128 v[152:155], v236 offset:3584\n" \
  "ds_read_b128 v[156:159], v236 offset:4096\n" \
  "ds_read_b128 v[200:203], v235 offset:6656\n" \
  "ds_read_b128 v[204:207], v235 offset:7168\n" \
  "s_waitcnt lgkmcnt(5)\n" \
  "v_fma_mix_f32 v218, v32, v144, 0 op_sel:[0,0,0] op_sel_hi:[0,1,0]\n" \
  "v_fma_mix_f32 v219, v48, v144, 0 op_sel:[0,1,0] op_sel_hi:[0,1,0]\n" \
  "v_fma_mix_f32 v218, v33, v145, v218 op_sel:[0,0,0] op_sel_hi:[0,1,0]\n" \
  "v_fma_mix_f32 v219, v49, v145, v219 op_sel:[0,1,0] op_sel_hi:[0,1,0]\n" \
  "v_fma_mix_f32 v218, v34, v146, v218 op_sel:[0,0,0] op_sel_hi:[0,1,0]\n" \
  "v_fma_mix_f32 v219, v50, v146, v219 op_sel:[0,1,0] op_sel_hi:[0,1,0]\n" \
  "v_fma_mix_f32 v218, v35, v147, v218 op_sel:[0,0,0] op_sel_hi:[0,1,0]\n" \
  "v_fma_mix_f32 v219, v51, v147, v219 op_sel:[0,1,0] op_sel_hi:[0,1,0]\n" \
  "ds_read_b128 v[144:147], v235 offset:7680\n" \
  "s_waitcnt lgkmcnt(5)\n" \
  "v_fma_mix_f32 v218, v36, v148, v218 op_sel:[0,0,0] op_sel_hi:[0,1,0]\n" \
  "v_fma_mix_f32 v219, v52, v148, v219 op_sel:[0,1,0] op_sel_hi:[0,1,0]\n" \
  "v_fma_mix_f32 v218, v37, v149, v218 op_sel:[0,0,0] op_sel_hi:[0,1,0]\n" \
  "v_fma_mix_f32 v219, v53, v149, v219 op_sel:[0,1,0] op_sel_hi:[0,1,0]\n" \
  "v_fma_mix_f32 v218, v38, v150, v218 op_sel:[0,0,0] op_sel_hi:[0,1,0]\n" \
  "v_fma_mix_f32 v219, v54, v150, v219 op_sel:[0,1,0] op_sel_hi:[0,1,0]\n" \
  "v_fma_mix_f32 v218, v39, v151, v218 op_sel:[0,0,0] op_sel_hi:[0,1,0]\n" \
  "v_fma_mix_f32 v219, v55, v151, v219 op_sel:[0,1,0] op_sel_hi:[0,1,0]\n" \
  "ds_read_b128 v[148:151], v235 offset:8192\n" \
  "s_waitcnt lgkmcnt(5)\n" \
  "v_fma_mix_f32 v218, v40, v152, v218 op_sel:[0,0,0] op_sel_hi:[0,1,0]\n" \
  "v_fma_mix_f32 v219, v56, v152, v219 op_sel:[0,1,0] op_sel_hi:[0,1,0]\n" \
  "v_fma_mix_f32 v218, v41, v153, v218 op_sel:[0,0,0] op_sel_hi:[0,1,0]\n" \
  "v_fma_mix_f32 v219, v57, v153, v219 op_sel:[0,1,0] op_sel_hi:[0,1,0]\n" \
  "v_fma_mix_f32 v218, v42, v154, v218 op_sel:[0,0,0] op_sel_hi:[0,1,0]\n" \
  "v_fma_mix_f32 v219, v58, v154, v219 op_sel:[0,1,0] op_sel_hi:[0,1,0]\n" \
  "v_fma_mix_f32 v218, v43, v155, v218 op_sel:[0,0,0] op_sel_hi:[0,1,0]\n" \
  "v_fma_mix_f32 v219, v59, v155, v219 op_sel:[0,1,0] op_sel_hi:[0,1,0]\n" \
  "ds_read_b128 v[152:155], v237 offset:2560\n" \
  "s_waitcnt lgkmcnt(5)\n" \
  "v_fma_mix_f32 v218, v44, v156, v218 op_sel:[0,0,0] op_sel_hi:[0,1,0]\n" \
  "v_fma_mix_f32 v219, v60, v156, v219 op_sel:[0,1,0] op_sel_hi:[0,1,0]\n" \
  "v_fma_mix_f32 v218, v45, v157, v218 op_sel:[0,0,0] op_sel_hi:[0,1,0]\n" \
  "v_fma_mix_f32 v219, v61, v157, v219 op_sel:[0,1,0] op_sel_hi:[0,1,0]\n" \
  "v_fma_mix_f32 v218, v46, v158, v218 op_sel:[0,0,0] op_sel_hi:[0,1,0]\n" \
  "v_fma_mix_f32 v219, v62, v158, v219 op_sel:[0,1,0] op_sel_hi:[0,1,0]\n" \
  "v_fma_mix_f32 v218, v47, v159, v218 op_sel:[0,0,0] op_sel_hi:[0,1,0]\n" \
  "v_fma_mix_f32 v219, v63, v159, v219 op_sel:[0,1,0] op_sel_hi:[0,1,0]\n" \
  "ds_read_b128 v[156:159], v237 offset:3072\n" \
  "v_mfma_f32_32x32x4_2b_f16 v[32:63], v[228:229], v[228:229], 0\n" \
  "s_waitcnt lgkmcnt(5)\n" \
  "v_fma_mix_f32 v216, v0, v200, 0 op_sel:[0,0,0] op_sel_hi:[0,1,0]\n" \
  "v_fma_mix_f32 v217, v16, v200, 0 op_sel:[0,1,0] op_sel_hi:[0,1,0]\n" \
  "v_fma_mix_f32 v208, v64, v200, 0 op_sel:[0,0,0] op_sel_hi:[0,1,0]\n" \
  "v_fma_mix_f32 v209, v80, v200, 0 op_sel:[0,1,0] op_sel_hi:[0,1,0]\n" \
  "v_fma_mix_f32 v216, v1, v201, v216 op_sel:[0,0,0] op_sel_hi:[0,1,0]\n" \
  "v_fma_mix_f32 v217, v17, v201, v217 op_sel:[0,1,0] op_sel_hi:[0,1,0]\n" \
  "v_fma_mix_f32 v208, v65, v201, v208 op_sel:[0,0,0] op_sel_hi:[0,1,0]\n" \
  "v_fma_mix_f32 v209, v81, v201, v209 op_sel:[0,1,0] op_sel_hi:[0,1,0]\n" \
  "v_fma_mix_f32 v216, v2, v202, v216 op_sel:[0,0,0] op_sel_hi:[0,1,0]\n" \
  "v_fma_mix_f32 v217, v18, v202, v217 op_sel:[0,1,0] op_sel_hi:[0,1,0]\n" \
  "v_fma_mix_f32 v208, v66, v202, v208 op_sel:[0,0,0] op_sel_hi:[0,1,0]\n" \
  "v_fma_mix_f32 v209, v82, v202, v209 op_sel:[0,1,0] op_sel_hi:[0,1,0]\n" \
  "v_fma_mix_f32 v216, v3, v203, v216 op_sel:[0,0,0] op_sel_hi:[0,1,0]\n" \
  "v_fma_mix_f32 v217, v19, v203, v217 op_sel:[0,1,0] op_sel_hi:[0,1,0]\n" \
  "v_fma_mix_f32 v208, v67, v203, v208 op_sel:[0,0,0] op_sel_hi:[0,1,0]\n" \
  "v_fma_mix_f32 v209, v83, v203, v209 op_sel:[0,1,0] op_sel_hi:[0,1,0]\n" \
  "ds_read_b128 v[200:203], v237 offset:3584\n" \
  "s_waitcnt lgkmcnt(5)\n" \
  "v_fma_mix_f32 v216, v4, v204, v216 op_sel:[0,0,0] op_sel_hi:[0,1,0]\n" \
  "v_fma_mix_f32 v217, v20, v204, v217 op_sel:[0,1,0] op_sel_hi:[0,1,0]\n" \
  "v_fma_mix_f32 v208, v68, v204, v208 op_sel:[0,0,0] op_sel_hi:[0,1,0]\n" \
  "v_fma_mix_f32 v209, v84, v204, v209 op_sel:[0,1,0] op_sel_hi:[0,1,0]\n" \
  "v_fma_mix_f32 v216, v5, v205, v216 op_sel:[0,0,0] op_sel_hi:[0,1,0]\n" \
  "v_fma_mix_f32 v217, v21, v205, v217 op_sel:[0,1,0] op_sel_hi:[0,1,0]\n" \
  "v_fma_mix_f32 v208, v69, v205, v208 op_sel:[0,0,0] op_sel_hi:[0,1,0]\n" \
  "v_fma_mix_f32 v209, v85, v205, v209 op_sel:[0,1,0] op_sel_hi:[0,1,0]\n" \
  "v_fma_mix_f32 v216, v6, v206, v216 op_sel:[0,0,0] op_sel_hi:[0,1,0]\n" \
  "v_fma_mix_f32 v217, v22, v206, v217 op_sel:[0,1,0] op_sel_hi:[0,1,0]\n" \
  "v_fma_mix_f32 v208, v70, v206, v208 op_sel:[0,0,0] op_sel_hi:[0,1,0]\n" \
  "v_fma_mix_f32 v209, v86, v206, v209 op_sel:[0,1,0] op_sel_hi:[0,1,0]\n" \
  "v_fma_mix_f32 v216, v7, v207, v216 op_sel:[0,0,0] op_sel_hi:[0,1,0]\n" \
  "v_fma_mix_f32 v217, v23, v207, v217 op_sel:[0,1,0] op_sel_hi:[0,1,0]\n" \
  "v_fma_mix_f32 v208, v71, v207, v208 op_sel:[0,0,0] op_sel_hi:[0,1,0]\n" \
  "v_fma_mix_f32 v209, v87, v207, v209 op_sel:[0,1,0] op_sel_hi:[0,1,0]\n" \
  "ds_read_b128 v[204:207], v237 offset:4096\n" \
  "s_waitcnt lgkmcnt(5)\n" \
  "v_fma_mix_f32 v216, v8, v144, v216 op_sel:[0,0,0] op_sel_hi:[0,1,0]\n" \
  "v_fma_mix_f32 v217, v24, v144, v217 op_sel:[0,1,0] op_sel_hi:[0,1,0]\n" \
  "v_fma_mix_f32 v208, v72, v144, v208 op_sel:[0,0,0] op_sel_hi:[0,1,0]\n" \
  "v_fma_mix_f32 v209, v88, v144, v209 op_sel:[0,1,0] op_sel_hi:[0,1,0]\n" \
  "v_fma_mix_f32 v216, v9, v145, v216 op_sel:[0,0,0] op_sel_hi:[0,1,0]\n" \
  "v_fma_mix_f32 v217, v25, v145, v217 op_sel:[0,1,0] op_sel_hi:[0,1,0]\n" \
  "v_fma_mix_f32 v208, v73, v145, v208 op_sel:[0,0,0] op_sel_hi:[0,1,0]\n" \
  "v_fma_mix_f32 v209, v89, v145, v209 op_sel:[0,1,0] op_sel_hi:[0,1,0]\n" \
  "v_fma_mix_f32 v216, v10, v146, v216 op_sel:[0,0,0] op_sel_hi:[0,1,0]\n" \
  "v_fma_mix_f32 v217, v26, v146, v217 op_sel:[0,1,0] op_sel_hi:[0,1,0]\n" \
  "v_fma_mix_f32 v208, v74, v146, v208 op_sel:[0,0,0] op_sel_hi:[0,1,0]\n" \
  "v_fma_mix_f32 v209, v90, v146, v209 op_sel:[0,1,0] op_sel_hi:[0,1,0]\n" \
  "v_fma_mix_f32 v216, v11, v147, v216 op_sel:[0,0,0] op_sel_hi:[0,1,0]\n" \
  "v_fma_mix_f32 v217, v27, v147, v217 op_sel:[0,1,0] op_sel_hi:[0,1,0]\n" \
  "v_fma_mix_f32 v208, v75, v147, v208 op_sel:[0,0,0] op_sel_hi:[0,1,0]\n" \
  "v_fma_mix_f32 v209, v91, v147, v209 op_sel:[0,1,0] op_sel_hi:[0,1,0]\n" \
  "s_waitcnt lgkmcnt(4)\n" \
  "v_fma_mix_f32 v216, v12, v148, v216 op_sel:[0,0,0] op_sel_hi:[0,1,0]\n" \
  "v_fma_mix_f32 v217, v28, v148, v217 op_sel:[0,1,0] op_sel_hi:[0,1,0]\n" \
  "v_fma_mix_f32 v208, v76, v148, v208 op_sel:[0,0,0] op_sel_hi:[0,1,0]\n" \
  "v_fma_mix_f32 v209, v92, v148, v209 op_sel:[0,1,0] op_sel_hi:[0,1,0]\n" \
  "v_fma_mix_f32 v216, v13, v149, v216 op_sel:[0,0,0] op_sel_hi:[0,1,0]\n" \
  "v_fma_mix_f32 v217, v29, v149, v217 op_sel:[0,1,0] op_sel_hi:[0,1,0]\n" \
  "v_fma_mix_f32 v208, v77, v149, v208 op_sel:[0,0,0] op_sel_hi:[0,1,0]\n" \
  "v_fma_mix_f32 v209, v93, v149, v209 op_sel:[0,1,0] op_sel_hi:[0,1,0]\n" \
  "v_fma_mix_f32 v216, v14, v150, v216 op_sel:[0,0,0] op_sel_hi:[0,1,0]\n" \
  "v_fma_mix_f32 v217, v30, v150, v217 op_sel:[0,1,0] op_sel_hi:[0,1,0]\n" \
  "v_fma_mix_f32 v208, v78, v150, v208 op_sel:[0,0,0] op_sel_hi:[0,1,0]\n" \
  "v_fma_mix_f32 v209, v94, v150, v209 op_sel:[0,1,0] op_sel_hi:[0,1,0]\n" \
  "v_fma_mix_f32 v216, v15, v151, v216 op_sel:[0,0,0] op_sel_hi:[0,1,0]\n" \
  "v_fma_mix_f32 v217, v31, v151, v217 op_sel:[0,1,0] op_sel_hi:[0,1,0]\n" \
  "v_fma_mix_f32 v208, v79, v151, v208 op_sel:[0,0,0] op_sel_hi:[0,1,0]\n" \
  "v_fma_mix_f32 v209, v95, v151, v209 op_sel:[0,1,0] op_sel_hi:[0,1,0]\n" \
  "v_mfma_f32_32x32x4_2b_f16 v[0:31], v[228:229], v[228:229], 0\n" \
  "v_mfma_f32_32x32x4_2b_f16 v[64:95], v[228:229], v[228:229], 0\n" \
  "s_waitcnt lgkmcnt(3)\n" \
  "v_fma_mix_f32 v210, v96, v152, 0 op_sel:[0,0,0] op_sel_hi:[0,1,0]\n" \
  "v_fma_mix_f32 v211, v112, v152, 0 op_sel:[0,1,0] op_sel_hi:[0,1,0]\n" \
  "v_fma_mix_f32 v210, v97, v153, v210 op_sel:[0,0,0] op_sel_hi:[0,1,0]\n" \
  "v_fma_mix_f32 v211, v113, v153, v211 op_sel:[0,1,0] op_sel_hi:[0,1,0]\n" \
  "v_fma_mix_f32 v210, v98, v154, v210 op_sel:[0,0,0] op_sel_hi:[0,1,0]\n" \
  "v_fma_mix_f32 v211, v114, v154, v211 op_sel:[0,1,0] op_sel_hi:[0,1,0]\n" \
  "v_fma_mix_f32 v210, v99, v155, v210 op_sel:[0,0,0] op_sel_hi:[0,1,0]\n" \
  "v_fma_mix_f32 v211, v115, v155, v211 op_sel:[0,1,0] op_sel_hi:[0,1,0]\n" \
  "s_waitcnt lgkmcnt(2)\n" \
  "v_fma_mix_f32 v210, v100, v156, v210 op_sel:[0,0,0] op_sel_hi:[0,1,0]\n" \
  "v_fma_mix_f32 v211, v116, v156, v211 op_sel:[0,1,0] op_sel_hi:[0,1,0]\n" \
  "v_fma_mix_f32 v210, v101, v157, v210 op_sel:[0,0,0] op_sel_hi:[0,1,0]\n" \
  "v_fma_mix_f32 v211, v117, v157, v211 op_sel:[0,1,0] op_sel_hi:[0,1,0]\n" \
  "v_fma_mix_f32 v210, v102, v158, v210 op_sel:[0,0,0] op_sel_hi:[0,1,0]\n" \
  "v_fma_mix_f32 v211, v118, v158, v211 op_sel:[0,1,0] op_sel_hi:[0,1,0]\n" \
  "v_fma_mix_f32 v210, v103, v159, v210 op_sel:[0,0,0] op_sel_hi:[0,1,0]\n" \
  "v_fma_mix_f32 v211, v119, v159, v211 op_sel:[0,1,0] op_sel_hi:[0,1,0]\n" \
  "s_waitcnt lgkmcnt(1)\n" \
  "v_fma_mix_f32 v210, v104, v200, v210 op_sel:[0,0,0] op_sel_hi:[0,1,0]\n" \
  "v_fma_mix_f32 v211, v120, v200, v211 op_sel:[0,1,0] op_sel_hi:[0,1,0]\n" \
  "v_fma_mix_f32 v210, v105, v201, v210 op_sel:[0,0,0] op_sel_hi:[0,1,0]\n" \
  "v_fma_mix_f32 v211, v121, v201, v211 op_sel:[0,1,0] op_sel_hi:[0,1,0]\n" \
  "v_fma_mix_f32 v210, v106, v202, v210 op_sel:[0,0,0] op_sel_hi:[0,1,0]\n" \
  "v_fma_mix_f32 v211, v122, v202, v211 op_sel:[0,1,0] op_sel_hi:[0,1,0]\n" \
  "v_fma_mix_f32 v210, v107, v203, v210 op_sel:[0,0,0] op_sel_hi:[0,1,0]\n" \
  "v_fma_mix_f32 v211, v123, v203, v211 op_sel:[0,1,0] op_sel_hi:[0,1,0]\n" \
  "s_waitcnt lgkmcnt(0)\n" \
  "v_fma_mix_f32 v210, v108, v204, v210 op_sel:[0,0,0] op_sel_hi:[0,1,0]\n" \
  "v_fma_mix_f32 v211, v124, v204, v211 op_sel:[0,1,0] op_sel_hi:[0,1,0]\n" \
  "v_fma_mix_f32 v210, v109, v205, v210 op_sel:[0,0,0] op_sel_hi:[0,1,0]\n" \
  "v_fma_mix_f32 v211, v125, v205, v211 op_sel:[0,1,0] op_sel_hi:[0,1,0]\n" \
  "v_fma_mix_f32 v210, v110, v206, v210 op_sel:[0,0,0] op_sel_hi:[0,1,0]\n" \
  "v_fma_mix_f32 v211, v126, v206, v211 op_sel:[0,1,0] op_sel_hi:[0,1,0]\n" \
  "v_fma_mix_f32 v210, v111, v207, v210 op_sel:[0,0,0] op_sel_hi:[0,1,0]\n" \
  "v_fma_mix_f32 v211, v127, v207, v211 op_sel:[0,1,0] op_sel_hi:[0,1,0]\n" \
  "v_mfma_f32_32x32x4_2b_f16 v[96:127], v[228:229], v[228:229], 0\n" \
  "v_cvt_pk_bf16_f32 v220, v216, v217\n" \
  "v_cvt_pk_bf16_f32 v221, v218, v219\n" \
  "v_cvt_pk_bf16_f32 v222, v208, v209\n" \
  "v_cvt_pk_bf16_f32 v223, v210, v211\n" \
  "ds_write2_b32 v240, v220, v222 offset0:64 offset1:96\n" \
  "ds_write_b32 v240, v221 offset:1280\n" \
  "ds_write_b32 v240, v223 offset:1408\n" \
  "s_sub_u32 s92, s92, 0x200\n" \
  "s_subb_u32 s93, s93, 0\n" \
  "s_bitcmp0_b32 s66, 16\n" \
  "s_cbranch_scc1 Lend_%=\n" \
  "s_setprio 1\n" \
  "s_cmp_eq_u32 %[gen], 0\n" \
  "s_waitcnt lgkmcnt(0)\n" \
  "global_load_dwordx4 v[220:223], v233, s[92:93] offset:-512\n" \
  "s_cbranch_scc1 Lret1_%=\n" \
  "s_add_u32 s94, s92, 0x2000000\n" \
  "s_addc_u32 s95, s93, 0\n" \
  "global_load_dwordx2 v[230:231], v233, s[94:95] offset:-512\n" \
  "s_branch Lret1_%=\n" \
  "Lpc1_%=:\n" \
  "s_mov_b32 m0, s81\n" \
  "s_bitcmp0_b32 s73, 4\n" \
  "s_cselect_b32 exec_hi, -1, 0\n" \
  "global_load_lds_dwordx4 v224, s[74:75]\n" \
  "s_mov_b32 exec_hi, -1\n" \
  "s_add_u32 s81, s81, 0x400\n" \
  "s_lshr_b32 s73, s73, 1\n" \
  "s_mov_b32 s88, s80\n" \
  "v_mov_b32 v224, v225\n" \
  "v_mov_b32 v225, v226\n" \
  "v_mov_b32 v226, v227\n" \
  "s_branch Lpr1_%=\n" \
  "Lwt1_%=:\n" \
  "s_bcnt1_i32_b32 s82, s91\n" \
  "s_cmp_eq_u32 s82, 3\n" \
  "s_waitcnt vmcnt(35)\n" \
  "s_cbranch_scc1 Lwr1_%=\n" \
  "s_cmp_eq_u32 s82, 2\n" \
  "s_waitcnt vmcnt(34)\n" \
  "s_cbranch_scc1 Lwr1_%=\n" \
  "s_waitcnt vmcnt(33)\n" \
  "s_branch Lwr1_%=\n" \
  ".p2align 3\n" \
  "Lepi2_%=:\n" \
  "s_setprio 0\n" \
  "s_bitcmp1_b32 s67, 18\n" \
  "s_cbranch_scc1 Lep12_%=\n" \
  "s_cmp_eq_u32 %[gen], 0\n" \
  "s_cbranch_scc1 Lg02_%=\n" \
  "ds_read_b128 v[160:163], v232 offset:8704\n" \
  "ds_read_b128 v[164:167], v232 offset:9216\n" \
  "ds_read_b128 v[168:171], v232 offset:9728\n" \
  "ds_read_b128 v[172:175], v232 offset:10240\n" \
  "s_waitcnt lgkmcnt(3)\n" \
  "v_fma_mix_f32 v230, v64, v160, 0 op_sel:[0,0,0] op_sel_hi:[0,1,0]\n" \
  "v_fma_mix_f32 v231, v80, v160, 0 op_sel:[0,1,0] op_sel_hi:[0,1,0]\n" \
  "v_fma_mix_f32 v230, v65, v161, v230 op_sel:[0,0,0] op_sel_hi:[0,1,0]\n" \
  "v_fma_mix_f32 v231, v81, v161, v231 op_sel:[0,1,0] op_sel_hi:[0,1,0]\n" \
  "v_fma_mix_f32 v230, v66, v162, v230 op_sel:[0,0,0] op_sel_hi:[0,1,0]\n" \
  "v_fma_mix_f32 v231, v82, v162, v231 op_sel:[0,1,0] op_sel_hi:[0,1,0]\n" \
  "v_fma_mix_f32 v230, v67, v163, v230 op_sel:[0,0,0] op_sel_hi:[0,1,0]\n" \
  "v_fma_mix_f32 v231, v83, v163, v231 op_sel:[0,1,0] op_sel_hi:[0,1,0]\n" \
  "s_waitcnt lgkmcnt(2)\n" \
  "v_fma_mix_f32 v230, v68, v164, v230 op_sel:[0,0,0] op_sel_hi:[0,1,0]\n" \
  "v_fma_mix_f32 v231, v84, v164, v231 op_sel:[0,1,0] op_sel_hi:[0,1,0]\n" \
  "v_fma_mix_f32 v230, v69, v165, v230 op_sel:[0,0,0] op_sel_hi:[0,1,0]\n" \
  "v_fma_mix_f32 v231, v85, v165, v231 op_sel:[0,1,0] op_sel_hi:[0,1,0]\n" \
  "v_fma_mix_f32 v230, v70, v166, v230 op_sel:[0,0,0] op_sel_hi:[0,1,0]\n" \
  "v_fma_mix_f32 v231, v86, v166, v231 op_sel:[0,1,0] op_sel_hi:[0,1,0]\n" \
  "v_fma_mix_f32 v230, v71, v167, v230 op_sel:[0,0,0] op_sel_hi:[0,1,0]\n" \
  "v_fma_mix_f32 v231, v87, v167, v231 op_sel:[0,1,0] op_sel_hi:[0,1,0]\n" \
  "s_waitcnt lgkmcnt(1)\n" \
  "v_fma_mix_f32 v230, v72, v168, v230 op_sel:[0,0,0] op_sel_hi:[0,1,0]\n" \
  "v_fma_mix_f32 v231, v88, v168, v231 op_sel:[0,1,0] op_sel_hi:[0,1,0]\n" \
  "v_fma_mix_f32 v230, v73, v169, v230 op_sel:[0,0,0] op_sel_hi:[0,1,0]\n" \
  "v_fma_mix_f32 v231, v89, v169, v231 op_sel:[0,1,0] op_sel_hi:[0,1,0]\n" \
  "v_fma_mix_f32 v230, v74, v170, v230 op_sel:[0,0,0] op_sel_hi:[0,1,0]\n" \
  "v_fma_mix_f32 v231, v90, v170, v231 op_sel:[0,1,0] op_sel_hi:[0,1,0]\n" \
  "v_fma_mix_f32 v230, v75, v171, v230 op_sel:[0,0,0] op_sel_hi:[0,1,0]\n" \
  "v_fma_mix_f32 v231, v91, v171, v231 op_sel:[0,1,0] op_sel_hi:[0,1,0]\n" \
  "s_waitcnt lgkmcnt(0)\n" \
  "v_fma_mix_f32 v230, v76, v172, v230 op_sel:[0,0,0] op_sel_hi:[0,1,0]\n" \
  "v_fma_mix_f32 v231, v92, v172, v231 op_sel:[0,1,0] op_sel_hi:[0,1,0]\n" \
  "v_fma_mix_f32 v230, v77, v173, v230 op_sel:[0,0,0] op_sel_hi:[0,1,0]\n" \
  "v_fma_mix_f32 v231, v93, v173, v231 op_sel:[0,1,0] op_sel_hi:[0,1,0]\n" \
  "v_fma_mix_f32 v230, v78, v174, v230 op_sel:[0,0,0] op_sel_hi:[0,1,0]\n" \
  "v_fma_mix_f32 v231, v94, v174, v231 op_sel:[0,1,0] op_sel_hi:[0,1,0]\n" \
  "v_fma_mix_f32 v230, v79, v175, v230 op_sel:[0,0,0] op_sel_hi:[0,1,0]\n" \
  "v_fma_mix_f32 v231, v95, v175, v231 op_sel:[0,1,0] op_sel_hi:[0,1,0]\n" \
  "s_add_u32 s94, s92, 0x2000000\n" \
  "s_addc_u32 s95, s93, 0\n" \
  "global_store_dwordx2 v233, v[230:231], s[94:95]\n" \
  "Lg02_%=:\n" \
  "ds_read_b128 v[160:163], v233 offset:512\n" \
  "ds_read_b128 v[164:167], v233 offset:1024\n" \
  "ds_read_b128 v[168:171], v233 offset:1536\n" \
  "ds_read_b128 v[172:175], v233 offset:2048\n" \
  "ds_read_b128 v[192:195], v232 offset:4608\n" \
  "ds_read_b128 v[196:199], v232 offset:5120\n" \
  "s_waitcnt lgkmcnt(5)\n" \
  "v_fma_mix_f32 v216, v0, v160, 0 op_sel:[0,0,0] op_sel_hi:[0,1,0]\n" \
  "v_fma_mix_f32 v217, v16, v160, 0 op_sel:[0,1,0] op_sel_hi:[0,1,0]\n" \
  "v_fma_mix_f32 v216, v1, v161, v216 op_sel:[0,0,0] op_sel_hi:[0,1,0]\n" \
  "v_fma_mix_f32 v217, v17, v161, v217 op_sel:[0,1,0] op_sel_hi:[0,1,0]\n" \
  "v_fma_mix_f32 v216, v2, v162, v216 op_sel:[0,0,0] op_sel_hi:[0,1,0]\n" \
  "v_fma_mix_f32 v217, v18, v162, v217 op_sel:[0,1,0] op_sel_hi:[0,1,0]\n" \
  "v_fma_mix_f32 v216, v3, v163, v216 op_sel:[0,0,0] op_sel_hi:[0,1,0]\n" \
  "v_fma_mix_f32 v217, v19, v163, v217 op_sel:[0,1,0] op_sel_hi:[0,1,0]\n" \
  "ds_read_b128 v[160:163], v232 offset:5632\n" \
  "s_waitcnt lgkmcnt(5)\n" \
  "v_fma_mix_f32 v216, v4, v164, v216 op_sel:[0,0,0] op_sel_hi:[0,1,0]\n" \
  "v_fma_mix_f32 v217, v20, v164, v217 op_sel:[0,1,0] op_sel_hi:[0,1,0]\n" \
  "v_fma_mix_f32 v216, v5, v165, v216 op_sel:[0,0,0] op_sel_hi:[0,1,0]\n" \
  "v_fma_mix_f32 v217, v21, v165, v217 op_sel:[0,1,0] op_sel_hi:[0,1,0]\n" \
  "v_fma_mix_f32 v216, v6, v166, v216 op_sel:[0,0,0] op_sel_hi:[0,1,0]\n" \
  "v_fma_mix_f32 v217, v22, v166, v217 op_sel:[0,1,0] op_sel_hi:[0,1,0]\n" \
  "v_fma_mix_f32 v216, v7, v167, v216 op_sel:[0,0,0] op_sel_hi:[0,1,0]\n" \
  "v_fma_mix_f32 v217, v23, v167, v217 op_sel:[0,1,0] op_sel_hi:[0,1,0]\n" \
  "ds_read_b128 v[164:167], v232 offset:6144\n" \
  "s_waitcnt lgkmcnt(5)\n" \
  "v_fma_mix_f32 v216, v8, v168, v216 op_sel:[0,0,0] op_sel_hi:[0,1,0]\n" \
  "v_fma_mix_f32 v217, v24, v168, v217 op_sel:[0,1,0] op_sel_hi:[0,1,0]\n" \
  "v_fma_mix_f32 v216, v9, v169, v216 op_sel:[0,0,0] op_sel_hi:[0,1,0]\n" \
  "v_fma_mix_f32 v217, v25, v169, v217 op_sel:[0,1,0] op_sel_hi:[0,1,0]\n" \
  "v_fma_mix_f32 v216, v10, v170, v216 op_sel:[0,0,0] op_sel_hi:[0,1,0]\n" \
  "v_fma_mix_f32 v217, v26, v170, v217 op_sel:[0,1,0] op_sel_hi:[0,1,0]\n" \
  "v_fma_mix_f32 v216, v11, v171, v216 op_sel:[0,0,0] op_sel_hi:[0,1,0]\n" \
  "v_fma_mix_f32 v217, v27, v171, v217 op_sel:[0,1,0] op_sel_hi:[0,1,0]\n" \
  "ds_read_b128 v[168:171], v234 offset:2560\n" \
  "s_waitcnt lgkmcnt(5)\n" \
  "v_fma_mix_f32 v216, v12, v172, v216 op_sel:[0,0,0] op_sel_hi:[0,1,0]\n" \
  "v_fma_mix_f32 v217, v28, v172, v217 op_sel:[0,1,0] op_sel_hi:[0,1,0]\n" \
  "v_fma_mix_f32 v216, v13, v173, v216 op_sel:[0,0,0] op_sel_hi:[0,1,0]\n" \
  "v_fma_mix_f32 v217, v29, v173, v217 op_sel:[0,1,0] op_sel_hi:[0,1,0]\n" \
  "v_fma_mix_f32 v216, v14, v174, v216 op_sel:[0,0,0] op_sel_hi:[0,1,0]\n" \
  "v_fma_mix_f32 v217, v30, v174, v217 op_sel:[0,1,0] op_sel_hi:[0,1,0]\n" \
  "v_fma_mix_f32 v216, v15, v175, v216 op_sel:[0,0,0] op_sel_hi:[0,1,0]\n" \
  "v_fma_mix_f32 v217, v31, v175, v217 op_sel:[0,1,0] op_sel_hi:[0,1,0]\n" \
  "ds_read_b128 v[172:175], v234 offset:3072\n" \
  "v_mfma_f32_32x32x4_2b_f16 v[0:31], v[228:229], v[228:229], 0\n" \
  "s_waitcnt lgkmcnt(5)\n" \
  "v_fma_mix_f32 v218, v32, v192, 0 op_sel:[0,0,0] op_sel_hi:[0,1,0]\n" \
  "v_fma_mix_f32 v219, v48, v192, 0 op_sel:[0,1,0] op_sel_hi:[0,1,0]\n" \
  "v_fma_mix_f32 v218, v33, v193, v218 op_sel:[0,0,0] op_sel_hi:[0,1,0]\n" \
  "v_fma_mix_f32 v219, v49, v193, v219 op_sel:[0,1,0] op_sel_hi:[0,1,0]\n" \
  "v_fma_mix_f32 v218, v34, v194, v218 op_sel:[0,0,0] op_sel_hi:[0,1,0]\n" \
  "v_fma_mix_f32 v219, v50, v194, v219 op_sel:[0,1,0] op_sel_hi:[0,1,0]\n" \
  "v_fma_mix_f32 v218, v35, v195, v218 op_sel:[0,0,0] op_sel_hi:[0,1,0]\n" \
  "v_fma_mix_f32 v219, v51, v195, v219 op_sel:[0,1,0] op_sel_hi:[0,1,0]\n" \
  "ds_read_b128 v[192:195], v234 offset:3584\n" \
  "s_waitcnt lgkmcnt(5)\n" \
  "v_fma_mix_f32 v218, v36, v196, v218 op_sel:[0,0,0] op_sel_hi:[0,1,0]\n" \
  "v_fma_mix_f32 v219, v52, v196, v219 op_sel:[0,1,0] op_sel_hi:[0,1,0]\n" \
  "v_fma_mix_f32 v218, v37, v197, v218 op_sel:[0,0,0] op_sel_hi:[0,1,0]\n" \
  "v_fma_mix_f32 v219, v53, v197, v219 op_sel:[0,1,0] op_sel_hi:[0,1,0]\n" \
  "v_fma_mix_f32 v218, v38, v198, v218 op_sel:[0,0,0] op_sel_hi:[0,1,0]\n" \
  "v_fma_mix_f32 v219, v54, v198, v219 op_sel:[0,1,0] op_sel_hi:[0,1,0]\n" \
  "v_fma_mix_f32 v218, v39, v199, v218 op_sel:[0,0,0] op_sel_hi:[0,1,0]\n" \
  "v_fma_mix_f32 v219, v55, v199, v219 op_sel:[0,1,0] op_sel_hi:[0,1,0]\n" \
  "ds_read_b128 v[196:199], v234 offset:4096\n" \
  "s_waitcnt lgkmcnt(5)\n" \
  "v_fma_mix_f32 v218, v40, v160, v218 op_sel:[0,0,0] op_sel_hi:[0,1,0]\n" \
  "v_fma_mix_f32 v219, v56, v160, v219 op_sel:[0,1,0] op_sel_hi:[0,1,0]\n" \
  "v_fma_mix_f32 v218, v41, v161, v218 op_sel:[0,0,0] op_sel_hi:[0,1,0]\n" \
  "v_fma_mix_f32 v219, v57, v161, v219 op_sel:[0,1,0] op_sel_hi:[0,1,0]\n" \
  "v_fma_mix_f32 v218, v42, v162, v218 op_sel:[0,0,0] op_sel_hi:[0,1,0]\n" \
  "v_fma_mix_f32 v219, v58, v162, v219 op_sel:[0,1,0] op_sel_hi:[0,1,0]\n" \
  "v_fma_mix_f32 v218, v43, v163, v218 op_sel:[0,0,0] op_sel_hi:[0,1,0]\n" \
  "v_fma_mix_f32 v219, v59, v163, v219 op_sel:[0,1,0] op_sel_hi:[0,1,0]\n" \
  "ds_read_b128 v[160:163], v232 offset:8704\n" \
  "s_waitcnt lgkmcnt(5)\n" \
  "v_fma_mix_f32 v218, v44, v164, v218 op_sel:[0,0,0] op_sel_hi:[0,1,0]\n" \
  "v_fma_mix_f32 v219, v60, v164, v219 op_sel:[0,1,0] op_sel_hi:[0,1,0]\n" \
  "v_fma_mix_f32 v218, v45, v165, v218 op_sel:[0,0,0] op_sel_hi:[0,1,0]\n" \
  "v_fma_mix_f32 v219, v61, v165, v219 op_sel:[0,1,0] op_sel_hi:[0,1,0]\n" \
  "v_fma_mix_f32 v218, v46, v166, v218 op_sel:[0,0,0] op_sel_hi:[0,1,0]\n" \
  "v_fma_mix_f32 v219, v62, v166, v219 op_sel:[0,1,0] op_sel_hi:[0,1,0]\n" \
  "v_fma_mix_f32 v218, v47, v167, v218 op_sel:[0,0,0] op_sel_hi:[0,1,0]\n" \
  "v_fma_mix_f32 v219, v63, v167, v219 op_sel:[0,1,0] op_sel_hi:[0,1,0]\n" \
  "ds_read_b128 v[164:167], v232 offset:9216\n" \
  "v_mfma_f32_32x32x4_2b_f16 v[32:63], v[228:229], v[228:229], 0\n" \
  "s_waitcnt lgkmcnt(5)\n" \
  "v_fma_mix_f32 v212, v64, v168, 0 op_sel:[0,0,0] op_sel_hi:[0,1,0]\n" \
  "v_fma_mix_f32 v213, v80, v168, 0 op_sel:[0,1,0] op_sel_hi:[0,1,0]\n" \
  "v_fma_mix_f32 v212, v65, v169, v212 op_sel:[0,0,0] op_sel_hi:[0,1,0]\n" \
  "v_fma_mix_f32 v213, v81, v169, v213 op_sel:[0,1,0] op_sel_hi:[0,1,0]\n" \
  "v_fma_mix_f32 v212, v66, v170, v212 op_sel:[0,0,0] op_sel_hi:[0,1,0]\n" \
  "v_fma_mix_f32 v213, v82, v170, v213 op_sel:[0,1,0] op_sel_hi:[0,1,0]\n" \
  "v_fma_mix_f32 v212, v67, v171, v212 op_sel:[0,0,0] op_sel_hi:[0,1,0]\n" \
  "v_fma_mix_f32 v213, v83, v171, v213 op_sel:[0,1,0] op_sel_hi:[0,1,0]\n" \
  "ds_read_b128 v[168:171], v232 offset:9728\n" \
  "s_waitcnt lgkmcnt(5)\n" \
  "v_fma_mix_f32 v212, v68, v172, v212 op_sel:[0,0,0] op_sel_hi:[0,1,0]\n" \
  "v_fma_mix_f32 v213, v84, v172, v213 op_sel:[0,1,0] op_sel_hi:[0,1,0]\n" \
  "v_fma_mix_f32 v212, v69, v173, v212 op_sel:[0,0,0] op_sel_hi:[0,1,0]\n" \
  "v_fma_mix_f32 v213, v85, v173, v213 op_sel:[0,1,0] op_sel_hi:[0,1,0]\n" \
  "v_fma_mix_f32 v212, v70, v174, v212 op_sel:[0,0,0] op_sel_hi:[0,1,0]\n" \
  "v_fma_mix_f32 v213, v86, v174, v213 op_sel:[0,1,0] op_sel_hi:[0,1,0]\n" \
  "v_fma_mix_f32 v212, v71, v175, v212 op_sel:[0,0,0] op_sel_hi:[0,1,0]\n" \
  "v_fma_mix_f32 v213, v87, v175, v213 op_sel:[0,1,0] op_sel_hi:[0,1,0]\n" \
  "ds_read_b128 v[172:175], v232 offset:10240\n" \
  "s_waitcnt lgkmcnt(5)\n" \
  "v_fma_mix_f32 v212, v72, v192, v212 op_sel:[0,0,0] op_sel_hi:[0,1,0]\n" \
  "v_fma_mix_f32 v213, v88, v192, v213 op_sel:[0,1,0] op_sel_hi:[0,1,0]\n" \
  "v_fma_mix_f32 v212, v73, v193, v212 op_sel:[0,0,0] op_sel_hi:[0,1,0]\n" \
  "v_fma_mix_f32 v213, v89, v193, v213 op_sel:[0,1,0] op_sel_hi:[0,1,0]\n" \
  "v_fma_mix_f32 v212, v74, v194, v212 op_sel:[0,0,0] op_sel_hi:[0,1,0]\n" \
  "v_fma_mix_f32 v213, v90, v194, v213 op_sel:[0,1,0] op_sel_hi:[0,1,0]\n" \
  "v_fma_mix_f32 v212, v75, v195, v212 op_sel:[0,0,0] op_sel_hi:[0,1,0]\n" \
  "v_fma_mix_f32 v213, v91, v195, v213 op_sel:[0,1,0] op_sel_hi:[0,1,0]\n" \
  "s_waitcnt lgkmcnt(4)\n" \
  "v_fma_mix_f32 v212, v76, v196, v212 op_sel:[0,0,0] op_sel_hi:[0,1,0]\n" \
  "v_fma_mix_f32 v213, v92, v196, v213 op_sel:[0,1,0] op_sel_hi:[0,1,0]\n" \
  "v_fma_mix_f32 v212, v77, v197, v212 op_sel:[0,0,0] op_sel_hi:[0,1,0]\n" \
  "v_fma_mix_f32 v213, v93, v197, v213 op_sel:[0,1,0] op_sel_hi:[0,1,0]\n" \
  "v_fma_mix_f32 v212, v78, v198, v212 op_sel:[0,0,0] op_sel_hi:[0,1,0]\n" \
  "v_fma_mix_f32 v213, v94, v198, v213 op_sel:[0,1,0] op_sel_hi:[0,1,0]\n" \
  "v_fma_mix_f32 v212, v79, v199, v212 op_sel:[0,0,0] op_sel_hi:[0,1,0]\n" \
  "v_fma_mix_f32 v213, v95, v199, v213 op_sel:[0,1,0] op_sel_hi:[0,1,0]\n" \
  "s_waitcnt lgkmcnt(3)\n" \
  "v_fma_mix_f32 v214, v96, v160, 0 op_sel:[0,0,0] op_sel_hi:[0,1,0]\n" \
  "v_fma_mix_f32 v215, v112, v160, 0 op_sel:[0,1,0] op_sel_hi:[0,1,0]\n" \
  "v_fma_mix_f32 v214, v97, v161, v214 op_sel:[0,0,0] op_sel_hi:[0,1,0]\n" \
  "v_fma_mix_f32 v215, v113, v161, v215 op_sel:[0,1,0] op_sel_hi:[0,1,0]\n" \
  "v_fma_mix_f32 v214, v98, v162, v214 op_sel:[0,0,0] op_sel_hi:[0,1,0]\n" \
  "v_fma_mix_f32 v215, v114, v162, v215 op_sel:[0,1,0] op_sel_hi:[0,1,0]\n" \
  "v_fma_mix_f32 v214, v99, v163, v214 op_sel:[0,0,0] op_sel_hi:[0,1,0]\n" \
  "v_fma_mix_f32 v215, v115, v163, v215 op_sel:[0,1,0] op_sel_hi:[0,1,0]\n" \
  "s_waitcnt lgkmcnt(2)\n" \
  "v_fma_mix_f32 v214, v100, v164, v214 op_sel:[0,0,0] op_sel_hi:[0,1,0]\n" \
  "v_fma_mix_f32 v215, v116, v164, v215 op_sel:[0,1,0] op_sel_hi:[0,1,0]\n" \
  "v_fma_mix_f32 v214, v101, v165, v214 op_sel:[0,0,0] op_sel_hi:[0,1,0]\n" \
  "v_fma_mix_f32 v215, v117, v165, v215 op_sel:[0,1,0] op_sel_hi:[0,1,0]\n" \
  "v_fma_mix_f32 v214, v102, v166, v214 op_sel:[0,0,0] op_sel_hi:[0,1,0]\n" \
  "v_fma_mix_f32 v215, v118, v166, v215 op_sel:[0,1,0] op_sel_hi:[0,1,0]\n" \
  "v_fma_mix_f32 v214, v103, v167, v214 op_sel:[0,0,0] op_sel_hi:[0,1,0]\n" \
  "v_fma_mix_f32 v215, v119, v167, v215 op_sel:[0,1,0] op_sel_hi:[0,1,0]\n" \
  "s_waitcnt lgkmcnt(1)\n" \
  "v_fma_mix_f32 v214, v104, v168, v214 op_sel:[0,0,0] op_sel_hi:[0,1,0]\n" \
  "v_fma_mix_f32 v215, v120, v168, v215 op_sel:[0,1,0] op_sel_hi:[0,1,0]\n" \
  "v_fma_mix_f32 v214, v105, v169, v214 op_sel:[0,0,0] op_sel_hi:[0,1,0]\n" \
  "v_fma_mix_f32 v215, v121, v169, v215 op_sel:[0,1,0] op_sel_hi:[0,1,0]\n" \
  "v_fma_mix_f32 v214, v106, v170, v214 op_sel:[0,0,0] op_sel_hi:[0,1,0]\n" \
  "v_fma_mix_f32 v215, v122, v170, v215 op_sel:[0,1,0] op_sel_hi:[0,1,0]\n" \
  "v_fma_mix_f32 v214, v107, v171, v214 op_sel:[0,0,0] op_sel_hi:[0,1,0]\n" \
  "v_fma_mix_f32 v215, v123, v171, v215 op_sel:[0,1,0] op_sel_hi:[0,1,0]\n" \
  "s_waitcnt lgkmcnt(0)\n" \
  "v_fma_mix_f32 v214, v108, v172, v214 op_sel:[0,0,0] op_sel_hi:[0,1,0]\n" \
  "v_fma_mix_f32 v215, v124, v172, v215 op_sel:[0,1,0] op_sel_hi:[0,1,0]\n" \
  "v_fma_mix_f32 v214, v109, v173, v214 op_sel:[0,0,0] op_sel_hi:[0,1,0]\n" \
  "v_fma_mix_f32 v215, v125, v173, v215 op_sel:[0,1,0] op_sel_hi:[0,1,0]\n" \
  "v_fma_mix_f32 v214, v110, v174, v214 op_sel:[0,0,0] op_sel_hi:[0,1,0]\n" \
  "v_fma_mix_f32 v215, v126, v174, v215 op_sel:[0,1,0] op_sel_hi:[0,1,0]\n" \
  "v_fma_mix_f32 v214, v111, v175, v214 op_sel:[0,0,0] op_sel_hi:[0,1,0]\n" \
  "v_fma_mix_f32 v215, v127, v175, v215 op_sel:[0,1,0] op_sel_hi:[0,1,0]\n" \
  "v_cvt_pk_bf16_f32 v220, v216, v217\n" \
  "v_cvt_pk_bf16_f32 v221, v218, v219\n" \
  "v_cvt_pk_bf16_f32 v222, v212, v213\n" \
  "v_cvt_pk_bf16_f32 v223, v214, v215\n" \
  "s_getpc_b64 s[70:71]\n" \
  "s_add_u32 s70, s70, 12\n" \
  "s_addc_u32 s71, s71, 0\n" \
  "s_branch Lasub_%=\n" \
  "v_mad_u32_u16 v100, s67, 64, %[c_a]\n" \
  "v_mad_u32_u16 v101, s67, 20, %[c_y]\n" \
  "ds_read_b64_tr_b16 v[216:217], v100\n" \
  "ds_read_b64_tr_b16 v[218:219], v100 offset:128\n" \
  "ds_read_b128 v[200:203], v101\n" \
  "ds_read_b128 v[204:207], v101 offset:32\n" \
  "global_store_dwordx4 v233, v[220:223], s[92:93] sc1\n" \
  "s_add_u32 s92, s92, 0x200\n" \
  "s_addc_u32 s93, s93, 0\n" \
  "v_mfma_f32_32x32x4_2b_f16 v[64:95], v[228:229], v[228:229], 0\n" \
  "v_mfma_f32_32x32x4_2b_f16 v[96:127], v[228:229], v[228:229], 0\n" \
  "s_waitcnt lgkmcnt(0)\n" \
  "v_pk_mul_f16 v200, v200, v216\n" \
  "v_pk_mul_f16 v201, v201, v217\n" \
  "v_pk_mul_f16 v202, v202, v218\n" \
  "v_pk_mul_f16 v203, v203, v219\n" \
  "v_pk_mul_f16 v204, v204, v216\n" \
  "v_pk_mul_f16 v205, v205, v217\n" \
  "v_pk_mul_f16 v206, v206, v218\n" \
  "v_pk_mul_f16 v207, v207, v219\n" \
  "s_setprio 1\n" \
  "s_branch Lret2_%=\n" \
  "Lep12_%=:\n" \
  "s_lshr_b32 s82, s67, 15\n" \
  "s_andn2_b32 s82, s82, 15\n" \
  "v_xad_u32 v240, s82, %[c_x], %[c_u]\n" \
  "s_waitcnt vmcnt(32)\n" \
  "ds_write2_b32 v240, v220, v222 offset1:32\n" \
  "ds_write_b32 v240, v221 offset:1024\n" \
  "ds_write_b32 v240, v223 offset:1152\n" \
  "s_cmp_eq_u32 %[gen], 0\n" \
  "s_cbranch_scc1 Lg12_%=\n" \
  "s_nop 7\n" \
  "ds_read_b128 v[160:163], v232 offset:8704\n" \
  "ds_read_b128 v[164:167], v232 offset:9216\n" \
  "ds_read_b128 v[168:171], v232 offset:9728\n" \
  "ds_read_b128 v[172:175], v232 offset:10240\n" \
  "s_waitcnt lgkmcnt(3)\n" \
  "v_fma_mix_f32 v216, v32, v160, 0 op_sel:[0,0,0] op_sel_hi:[0,1,0]\n" \
  "v_fma_mix_f32 v217, v48, v160, 0 op_sel:[0,1,0] op_sel_hi:[0,1,0]\n" \
  "v_fma_mix_f32 v218, v96, v160, 0 op_sel:[0,0,0] op_sel_hi:[0,1,0]\n" \
  "v_fma_mix_f32 v219, v112, v160, 0 op_sel:[0,1,0] op_sel_hi:[0,1,0]\n" \
  "v_fma_mix_f32 v216, v33, v161, v216 op_sel:[0,0,0] op_sel_hi:[0,1,0]\n" \
  "v_fma_mix_f32 v217, v49, v161, v217 op_sel:[0,1,0] op_sel_hi:[0,1,0]\n" \
  "v_fma_mix_f32 v218, v97, v161, v218 op_sel:[0,0,0] op_sel_hi:[0,1,0]\n" \
  "v_fma_mix_f32 v219, v113, v161, v219 op_sel:[0,1,0] op_sel_hi:[0,1,0]\n" \
  "v_fma_mix_f32 v216, v34, v162, v216 op_sel:[0,0,0] op_sel_hi:[0,1,0]\n" \
  "v_fma_mix_f32 v217, v50, v162, v217 op_sel:[0,1,0] op_sel_hi:[0,1,0]\n" \
  "v_fma_mix_f32 v218, v98, v162, v218 op_sel:[0,0,0] op_sel_hi:[0,1,0]\n" \
  "v_fma_mix_f32 v219, v114, v162, v219 op_sel:[0,1,0] op_sel_hi:[0,1,0]\n" \
  "v_fma_mix_f32 v216, v35, v163, v216 op_sel:[0,0,0] op_sel_hi:[0,1,0]\n" \
  "v_fma_mix_f32 v217, v51, v163, v217 op_sel:[0,1,0] op_sel_hi:[0,1,0]\n" \
  "v_fma_mix_f32 v218, v99, v163, v218 op_sel:[0,0,0] op_sel_hi:[0,1,0]\n" \
  "v_fma_mix_f32 v219, v115, v163, v219 op_sel:[0,1,0] op_sel_hi:[0,1,0]\n" \
  "s_waitcnt lgkmcnt(2)\n" \
  "v_fma_mix_f32 v216, v36, v164, v216 op_sel:[0,0,0] op_sel_hi:[0,1,0]\n" \
  "v_fma_mix_f32 v217, v52, v164, v217 op_sel:[0,1,0] op_sel_hi:[0,1,0]\n" \
  "v_fma_mix_f32 v218, v100, v164, v218 op_sel:[0,0,0] op_sel_hi:[0,1,0]\n" \
  "v_fma_mix_f32 v219, v116, v164, v219 op_sel:[0,1,0] op_sel_hi:[0,1,0]\n" \
  "v_fma_mix_f32 v216, v37, v165, v216 op_sel:[0,0,0] op_sel_hi:[0,1,0]\n" \
  "v_fma_mix_f32 v217, v53, v165, v217 op_sel:[0,1,0] op_sel_hi:[0,1,0]\n" \
  "v_fma_mix_f32 v218, v101, v165, v218 op_sel:[0,0,0] op_sel_hi:[0,1,0]\n" \
  "v_fma_mix_f32 v219, v117, v165, v219 op_sel:[0,1,0] op_sel_hi:[0,1,0]\n" \
  "v_fma_mix_f32 v216, v38, v166, v216 op_sel:[0,0,0] op_sel_hi:[0,1,0]\n" \
  "v_fma_mix_f32 v217, v54, v166, v217 op_sel:[0,1,0] op_sel_hi:[0,1,0]\n" \
  "v_fma_mix_f32 v218, v102, v166, v218 op_sel:[0,0,0] op_sel_hi:[0,1,0]\n" \
  "v_fma_mix_f32 v219, v118, v166, v219 op_sel:[0,1,0] op_sel_hi:[0,1,0]\n" \
  "v_fma_mix_f32 v216, v39, v167, v216 op_sel:[0,0,0] op_sel_hi:[0,1,0]\n" \
  "v_fma_mix_f32 v217, v55, v167, v217 op_sel:[0,1,0] op_sel_hi:[0,1,0]\n" \
  "v_fma_mix_f32 v218, v103, v167, v218 op_sel:[0,0,0] op_sel_hi:[0,1,0]\n" \
  "v_fma_mix_f32 v219, v119, v167, v219 op_sel:[0,1,0] op_sel_hi:[0,1,0]\n" \
  "s_waitcnt lgkmcnt(1)\n" \
  "v_fma_mix_f32 v216, v40, v168, v216 op_sel:[0,0,0] op_sel_hi:[0,1,0]\n" \
  "v_fma_mix_f32 v217, v56, v168, v217 op_sel:[0,1,0] op_sel_hi:[0,1,0]\n" \
  "v_fma_mix_f32 v218, v104, v168, v218 op_sel:[0,0,0] op_sel_hi:[0,1,0]\n" \
  "v_fma_mix_f32 v219, v120, v168, v219 op_sel:[0,1,0] op_sel_hi:[0,1,0]\n" \
  "v_fma_mix_f32 v216, v41, v169, v216 op_sel:[0,0,0] op_sel_hi:[0,1,0]\n" \
  "v_fma_mix_f32 v217, v57, v169, v217 op_sel:[0,1,0] op_sel_hi:[0,1,0]\n" \
  "v_fma_mix_f32 v218, v105, v169, v218 op_sel:[0,0,0] op_sel_hi:[0,1,0]\n" \
  "v_fma_mix_f32 v219, v121, v169, v219 op_sel:[0,1,0] op_sel_hi:[0,1,0]\n" \
  "v_fma_mix_f32 v216, v42, v170, v216 op_sel:[0,0,0] op_sel_hi:[0,1,0]\n" \
  "v_fma_mix_f32 v217, v58, v170, v217 op_sel:[0,1,0] op_sel_hi:[0,1,0]\n" \
  "v_fma_mix_f32 v218, v106, v170, v218 op_sel:[0,0,0] op_sel_hi:[0,1,0]\n" \
  "v_fma_mix_f32 v219, v122, v170, v219 op_sel:[0,1,0] op_sel_hi:[0,1,0]\n" \
  "v_fma_mix_f32 v216, v43, v171, v216 op_sel:[0,0,0] op_sel_hi:[0,1,0]\n" \
  "v_fma_mix_f32 v217, v59, v171, v217 op_sel:[0,1,0] op_sel_hi:[0,1,0]\n" \
  "v_fma_mix_f32 v218, v107, v171, v218 op_sel:[0,0,0] op_sel_hi:[0,1,0]\n" \
  "v_fma_mix_f32 v219, v123, v171, v219 op_sel:[0,1,0] op_sel_hi:[0,1,0]\n" \
  "s_waitcnt lgkmcnt(0)\n" \
  "v_fma_mix_f32 v216, v44, v172, v216 op_sel:[0,0,0] op_sel_hi:[0,1,0]\n" \
  "v_fma_mix_f32 v217, v60, v172, v217 op_sel:[0,1,0] op_sel_hi:[0,1,0]\n" \
  "v_fma_mix_f32 v218, v108, v172, v218 op_sel:[0,0,0] op_sel_hi:[0,1,0]\n" \
  "v_fma_mix_f32 v219, v124, v172, v219 op_sel:[0,1,0] op_sel_hi:[0,1,0]\n" \
  "v_fma_mix_f32 v216, v45, v173, v216 op_sel:[0,0,0] op_sel_hi:[0,1,0]\n" \
  "v_fma_mix_f32 v217, v61, v173, v217 op_sel:[0,1,0] op_sel_hi:[0,1,0]\n" \
  "v_fma_mix_f32 v218, v109, v173, v218 op_sel:[0,0,0] op_sel_hi:[0,1,0]\n" \
  "v_fma_mix_f32 v219, v125, v173, v219 op_sel:[0,1,0] op_sel_hi:[0,1,0]\n" \
  "v_fma_mix_f32 v216, v46, v174, v216 op_sel:[0,0,0] op_sel_hi:[0,1,0]\n" \
  "v_fma_mix_f32 v217, v62, v174, v217 op_sel:[0,1,0] op_sel_hi:[0,1,0]\n" \
  "v_fma_mix_f32 v218, v110, v174, v218 op_sel:[0,0,0] op_sel_hi:[0,1,0]\n" \
  "v_fma_mix_f32 v219, v126, v174, v219 op_sel:[0,1,0] op_sel_hi:[0,1,0]\n" \
  "v_fma_mix_f32 v216, v47, v175, v216 op_sel:[0,0,0] op_sel_hi:[0,1,0]\n" \
  "v_fma_mix_f32 v217, v63, v175, v217 op_sel:[0,1,0] op_sel_hi:[0,1,0]\n" \
  "v_fma_mix_f32 v218, v111, v175, v218 op_sel:[0,0,0] op_sel_hi:[0,1,0]\n" \
  "v_fma_mix_f32 v219, v127, v175, v219 op_sel:[0,1,0] op_sel_hi:[0,1,0]\n" \
  "v_cvt_pk_bf16_f32 v216, v216, v217\n" \
  "v_cvt_pk_bf16_f32 v218, v218, v219\n" \
  "v_cvt_pk_bf16_f32 v217, v230, v231\n" \
  "s_lshr_b32 s83, s67, 24\n" \
  "s_mul_i32 s83, s83, 160\n" \
  "s_and_b32 s82, s82, 0x70\n" \
  "v_xor_b32 v219, s82, %[c_x]\n" \
  "v_add_u32 v219, s83, v219\n" \
  "s_mov_b32 exec_hi, 0\n" \
  "ds_write_b32 v219, v216 offset:%[l_ys]\n" \
  "s_mov_b32 exec_hi, -1\n" \
  "s_mov_b32 exec_lo, 0\n" \
  "ds_write_b32 v219, v217 offset:%[l_ys]+128\n" \
  "ds_write_b32 v219, v218 offset:%[l_ys]+256\n" \
  "s_mov_b32 exec_lo, -1\n" \
  "s_waitcnt lgkmcnt(0)\n" \
  "Lg12_%=:\n" \
  "ds_read_b128 v[160:163], v236 offset:2560\n" \
  "ds_read_b128 v[164:167], v236 offset:3072\n" \
  "ds_read_b128 v[168:171], v236 offset:3584\n" \
  "ds_read_b128 v[172:175], v236 offset:4096\n" \
  "ds_read_b128 v[192:195], v235 offset:6656\n" \
  "ds_read_b128 v[196:199], v235 offset:7168\n" \
  "s_waitcnt lgkmcnt(5)\n" \
  "v_fma_mix_f32 v218, v32, v160, 0 op_sel:[0,0,0] op_sel_hi:[0,1,0]\n" \
  "v_fma_mix_f32 v219, v48, v160, 0 op_sel:[0,1,0] op_sel_hi:[0,1,0]\n" \
  "v_fma_mix_f32 v218, v33, v161, v218 op_sel:[0,0,0] op_sel_hi:[0,1,0]\n" \
  "v_fma_mix_f32 v219, v49, v161, v219 op_sel:[0,1,0] op_sel_hi:[0,1,0]\n" \
  "v_fma_mix_f32 v218, v34, v162, v218 op_sel:[0,0,0] op_sel_hi:[0,1,0]\n" \
  "v_fma_mix_f32 v219, v50, v162, v219 op_sel:[0,1,0] op_sel_hi:[0,1,0]\n" \
  "v_fma_mix_f32 v218, v35, v163, v218 op_sel:[0,0,0] op_sel_hi:[0,1,0]\n" \
  "v_fma_mix_f32 v219, v51, v163, v219 op_sel:[0,1,0] op_sel_hi:[0,1,0]\n" \
  "ds_read_b128 v[160:163], v235 offset:7680\n" \
  "s_waitcnt lgkmcnt(5)\n" \
  "v_fma_mix_f32 v218, v36, v164, v218 op_sel:[0,0,0] op_sel_hi:[0,1,0]\n" \
  "v_fma_mix_f32 v219, v52, v164, v219 op_sel:[0,1,0] op_sel_hi:[0,1,0]\n" \
  "v_fma_mix_f32 v218, v37, v165, v218 op_sel:[0,0,0] op_sel_hi:[0,1,0]\n" \
  "v_fma_mix_f32 v219, v53, v165, v219 op_sel:[0,1,0] op_sel_hi:[0,1,0]\n" \
  "v_fma_mix_f32 v218, v38, v166, v218 op_sel:[0,0,0] op_sel_hi:[0,1,0]\n" \
  "v_fma_mix_f32 v219, v54, v166, v219 op_sel:[0,1,0] op_sel_hi:[0,1,0]\n" \
  "v_fma_mix_f32 v218, v39, v167, v218 op_sel:[0,0,0] op_sel_hi:[0,1,0]\n" \
  "v_fma_mix_f32 v219, v55, v167, v219 op_sel:[0,1,0] op_sel_hi:[0,1,0]\n" \
  "ds_read_b128 v[164:167], v235 offset:8192\n" \
  "s_waitcnt lgkmcnt(5)\n" \
  "v_fma_mix_f32 v218, v40, v168, v218 op_sel:[0,0,0] op_sel_hi:[0,1,0]\n" \
  "v_fma_mix_f32 v219, v56, v168, v219 op_sel:[0,1,0] op_sel_hi:[0,1,0]\n" \
  "v_fma_mix_f32 v218, v41, v169, v218 op_sel:[0,0,0] op_sel_hi:[0,1,0]\n" \
  "v_fma_mix_f32 v219, v57, v169, v219 op_sel:[0,1,0] op_sel_hi:[0,1,0]\n" \
  "v_fma_mix_f32 v218, v42, v170, v218 op_sel:[0,0,0] op_sel_hi:[0,1,0]\n" \
  "v_fma_mix_f32 v219, v58, v170, v219 op_sel:[0,1,0] op_sel_hi:[0,1,0]\n" \
  "v_fma_mix_f32 v218, v43, v171, v218 op_sel:[0,0,0] op_sel_hi:[0,1,0]\n" \
  "v_fma_mix_f32 v219, v59, v171, v219 op_sel:[0,1,0] op_sel_hi:[0,1,0]\n" \
  "ds_read_b128 v[168:171], v237 offset:2560\n" \
  "s_waitcnt lgkmcnt(5)\n" \
  "v_fma_mix_f32 v218, v44, v172, v218 op_sel:[0,0,0] op_sel_hi:[0,1,0]\n" \
  "v_fma_mix_f32 v219, v60, v172, v219 op_sel:[0,1,0] op_sel_hi:[0,1,0]\n" \
  "v_fma_mix_f32 v218, v45, v173, v218 op_sel:[0,0,0] op_sel_hi:[0,1,0]\n" \
  "v_fma_mix_f32 v219, v61, v173, v219 op_sel:[0,1,0] op_sel_hi:[0,1,0]\n" \
  "v_fma_mix_f32 v218, v46, v174, v218 op_sel:[0,0,0] op_sel_hi:[0,1,0]\n" \
  "v_fma_mix_f32 v219, v62, v174, v219 op_sel:[0,1,0] op_sel_hi:[0,1,0]\n" \
  "v_fma_mix_f32 v218, v47, v175, v218 op_sel:[0,0,0] op_sel_hi:[0,1,0]\n" \
  "v_fma_mix_f32 v219, v63, v175, v219 op_sel:[0,1,0] op_sel_hi:[0,1,0]\n" \
  "ds_read_b128 v[172:175], v237 offset:3072\n" \
  "v_mfma_f32_32x32x4_2b_f16 v[32:63], v[228:229], v[228:229], 0\n" \
  "s_waitcnt lgkmcnt(5)\n" \
  "v_fma_mix_f32 v216, v0, v192, 0 op_sel:[0,0,0] op_sel_hi:[0,1,0]\n" \
  "v_fma_mix_f32 v217, v16, v192, 0 op_sel:[0,1,0] op_sel_hi:[0,1,0]\n" \
  "v_fma_mix_f32 v212, v64, v192, 0 op_sel:[0,0,0] op_sel_hi:[0,1,0]\n" \
  "v_fma_mix_f32 v213, v80, v192, 0 op_sel:[0,1,0] op_sel_hi:[0,1,0]\n" \
  "v_fma_mix_f32 v216, v1, v193, v216 op_sel:[0,0,0] op_sel_hi:[0,1,0]\n" \
  "v_fma_mix_f32 v217, v17, v193, v217 op_sel:[0,1,0] op_sel_hi:[0,1,0]\n" \
  "v_fma_mix_f32 v212, v65, v193, v212 op_sel:[0,0,0] op_sel_hi:[0,1,0]\n" \
  "v_fma_mix_f32 v213, v81, v193, v213 op_sel:[0,1,0] op_sel_hi:[0,1,0]\n" \
  "v_fma_mix_f32 v216, v2, v194, v216 op_sel:[0,0,0] op_sel_hi:[0,1,0]\n" \
  "v_fma_mix_f32 v217, v18, v194, v217 op_sel:[0,1,0] op_sel_hi:[0,1,0]\n" \
  "v_fma_mix_f32 v212, v66, v194, v212 op_sel:[0,0,0] op_sel_hi:[0,1,0]\n" \
  "v_fma_mix_f32 v213, v82, v194, v213 op_sel:[0,1,0] op_sel_hi:[0,1,0]\n" \
  "v_fma_mix_f32 v216, v3, v195, v216 op_sel:[0,0,0] op_sel_hi:[0,1,0]\n" \
  "v_fma_mix_f32 v217, v19, v195, v217 op_sel:[0,1,0] op_sel_hi:[0,1,0]\n" \
  "v_fma_mix_f32 v212, v67, v195, v212 op_sel:[0,0,0] op_sel_hi:[0,1,0]\n" \
  "v_fma_mix_f32 v213, v83, v195, v213 op_sel:[0,1,0] op_sel_hi:[0,1,0]\n" \
  "ds_read_b128 v[192:195], v237 offset:3584\n" \
  "s_waitcnt lgkmcnt(5)\n" \
  "v_fma_mix_f32 v216, v4, v196, v216 op_sel:[0,0,0] op_sel_hi:[0,1,0]\n" \
  "v_fma_mix_f32 v217, v20, v196, v217 op_sel:[0,1,0] op_sel_hi:[0,1,0]\n" \
  "v_fma_mix_f32 v212, v68, v196, v212 op_sel:[0,0,0] op_sel_hi:[0,1,0]\n" \
  "v_fma_mix_f32 v213, v84, v196, v213 op_sel:[0,1,0] op_sel_hi:[0,1,0]\n" \
  "v_fma_mix_f32 v216, v5, v197, v216 op_sel:[0,0,0] op_sel_hi:[0,1,0]\n" \
  "v_fma_mix_f32 v217, v21, v197, v217 op_sel:[0,1,0] op_sel_hi:[0,1,0]\n" \
  "v_fma_mix_f32 v212, v69, v197, v212 op_sel:[0,0,0] op_sel_hi:[0,1,0]\n" \
  "v_fma_mix_f32 v213, v85, v197, v213 op_sel:[0,1,0] op_sel_hi:[0,1,0]\n" \
  "v_fma_mix_f32 v216, v6, v198, v216 op_sel:[0,0,0] op_sel_hi:[0,1,0]\n" \
  "v_fma_mix_f32 v217, v22, v198, v217 op_sel:[0,1,0] op_sel_hi:[0,1,0]\n" \
  "v_fma_mix_f32 v212, v70, v198, v212 op_sel:[0,0,0] op_sel_hi:[0,1,0]\n" \
  "v_fma_mix_f32 v213, v86, v198, v213 op_sel:[0,1,0] op_sel_hi:[0,1,0]\n" \
  "v_fma_mix_f32 v216, v7, v199, v216 op_sel:[0,0,0] op_sel_hi:[0,1,0]\n" \
  "v_fma_mix_f32 v217, v23, v199, v217 op_sel:[0,1,0] op_sel_hi:[0,1,0]\n" \
  "v_fma_mix_f32 v212, v71, v199, v212 op_sel:[0,0,0] op_sel_hi:[0,1,0]\n" \
  "v_fma_mix_f32 v213, v87, v199, v213 op_sel:[0,1,0] op_sel_hi:[0,1,0]\n" \
  "ds_read_b128 v[196:199], v237 offset:4096\n" \
  "s_waitcnt lgkmcnt(5)\n" \
  "v_fma_mix_f32 v216, v8, v160, v216 op_sel:[0,0,0] op_sel_hi:[0,1,0]\n" \
  "v_fma_mix_f32 v217, v24, v160, v217 op_sel:[0,1,0] op_sel_hi:[0,1,0]\n" \
  "v_fma_mix_f32 v212, v72, v160, v212 op_sel:[0,0,0] op_sel_hi:[0,1,0]\n" \
  "v_fma_mix_f32 v213, v88, v160, v213 op_sel:[0,1,0] op_sel_hi:[0,1,0]\n" \
  "v_fma_mix_f32 v216, v9, v161, v216 op_sel:[0,0,0] op_sel_hi:[0,1,0]\n" \
  "v_fma_mix_f32 v217, v25, v161, v217 op_sel:[0,1,0] op_sel_hi:[0,1,0]\n" \
  "v_fma_mix_f32 v212, v73, v161, v212 op_sel:[0,0,0] op_sel_hi:[0,1,0]\n" \
  "v_fma_mix_f32 v213, v89, v161, v213 op_sel:[0,1,0] op_sel_hi:[0,1,0]\n" \
  "v_fma_mix_f32 v216, v10, v162, v216 op_sel:[0,0,0] op_sel_hi:[0,1,0]\n" \
  "v_fma_mix_f32 v217, v26, v162, v217 op_sel:[0,1,0] op_sel_hi:[0,1,0]\n" \
  "v_fma_mix_f32 v212, v74, v162, v212 op_sel:[0,0,0] op_sel_hi:[0,1,0]\n" \
  "v_fma_mix_f32 v213, v90, v162, v213 op_sel:[0,1,0] op_sel_hi:[0,1,0]\n" \
  "v_fma_mix_f32 v216, v11, v163, v216 op_sel:[0,0,0] op_sel_hi:[0,1,0]\n" \
  "v_fma_mix_f32 v217, v27, v163, v217 op_sel:[0,1,0] op_sel_hi:[0,1,0]\n" \
  "v_fma_mix_f32 v212, v75, v163, v212 op_sel:[0,0,0] op_sel_hi:[0,1,0]\n" \
  "v_fma_mix_f32 v213, v91, v163, v213 op_sel:[0,1,0] op_sel_hi:[0,1,0]\n" \
  "s_waitcnt lgkmcnt(4)\n" \
  "v_fma_mix_f32 v216, v12, v164, v216 op_sel:[0,0,0] op_sel_hi:[0,1,0]\n" \
  "v_fma_mix_f32 v217, v28, v164, v217 op_sel:[0,1,0] op_sel_hi:[0,1,0]\n" \
  "v_fma_mix_f32 v212, v76, v164, v212 op_sel:[0,0,0] op_sel_hi:[0,1,0]\n" \
  "v_fma_mix_f32 v213, v92, v164, v213 op_sel:[0,1,0] op_sel_hi:[0,1,0]\n" \
  "v_fma_mix_f32 v216, v13, v165, v216 op_sel:[0,0,0] op_sel_hi:[0,1,0]\n" \
  "v_fma_mix_f32 v217, v29, v165, v217 op_sel:[0,1,0] op_sel_hi:[0,1,0]\n" \
  "v_fma_mix_f32 v212, v77, v165, v212 op_sel:[0,0,0] op_sel_hi:[0,1,0]\n" \
  "v_fma_mix_f32 v213, v93, v165, v213 op_sel:[0,1,0] op_sel_hi:[0,1,0]\n" \
  "v_fma_mix_f32 v216, v14, v166, v216 op_sel:[0,0,0] op_sel_hi:[0,1,0]\n" \
  "v_fma_mix_f32 v217, v30, v166, v217 op_sel:[0,1,0] op_sel_hi:[0,1,0]\n" \
  "v_fma_mix_f32 v212, v78, v166, v212 op_sel:[0,0,0] op_sel_hi:[0,1,0]\n" \
  "v_fma_mix_f32 v213, v94, v166, v213 op_sel:[0,1,0] op_sel_hi:[0,1,0]\n" \
  "v_fma_mix_f32 v216, v15, v167, v216 op_sel:[0,0,0] op_sel_hi:[0,1,0]\n" \
  "v_fma_mix_f32 v217, v31, v167, v217 op_sel:[0,1,0] op_sel_hi:[0,1,0]\n" \
  "v_fma_mix_f32 v212, v79, v167, v212 op_sel:[0,0,0] op_sel_hi:[0,1,0]\n" \
  "v_fma_mix_f32 v213, v95, v167, v213 op_sel:[0,1,0] op_sel_hi:[0,1,0]\n" \
  "v_mfma_f32_32x32x4_2b_f16 v[0:31], v[228:229], v[228:229], 0\n" \
  "v_mfma_f32_32x32x4_2b_f16 v[64:95], v[228:229], v[228:229], 0\n" \
  "s_waitcnt lgkmcnt(3)\n" \
  "v_fma_mix_f32 v214, v96, v168, 0 op_sel:[0,0,0] op_sel_hi:[0,1,0]\n" \
  "v_fma_mix_f32 v215, v112, v168, 0 op_sel:[0,1,0] op_sel_hi:[0,1,0]\n" \
  "v_fma_mix_f32 v214, v97, v169, v214 op_sel:[0,0,0] op_sel_hi:[0,1,0]\n" \
  "v_fma_mix_f32 v215, v113, v169, v215 op_sel:[0,1,0] op_sel_hi:[0,1,0]\n" \
  "v_fma_mix_f32 v214, v98, v170, v214 op_sel:[0,0,0] op_sel_hi:[0,1,0]\n" \
  "v_fma_mix_f32 v215, v114, v170, v215 op_sel:[0,1,0] op_sel_hi:[0,1,0]\n" \
  "v_fma_mix_f32 v214, v99, v171, v214 op_sel:[0,0,0] op_sel_hi:[0,1,0]\n" \
  "v_fma_mix_f32 v215, v115, v171, v215 op_sel:[0,1,0] op_sel_hi:[0,1,0]\n" \
  "s_waitcnt lgkmcnt(2)\n" \
  "v_fma_mix_f32 v214, v100, v172, v214 op_sel:[0,0,0] op_sel_hi:[0,1,0]\n" \
  "v_fma_mix_f32 v215, v116, v172, v215 op_sel:[0,1,0] op_sel_hi:[0,1,0]\n" \
  "v_fma_mix_f32 v214, v101, v173, v214 op_sel:[0,0,0] op_sel_hi:[0,1,0]\n" \
  "v_fma_mix_f32 v215, v117, v173, v215 op_sel:[0,1,0] op_sel_hi:[0,1,0]\n" \
  "v_fma_mix_f32 v214, v102, v174, v214 op_sel:[0,0,0] op_sel_hi:[0,1,0]\n" \
  "v_fma_mix_f32 v215, v118, v174, v215 op_sel:[0,1,0] op_sel_hi:[0,1,0]\n" \
  "v_fma_mix_f32 v214, v103, v175, v214 op_sel:[0,0,0] op_sel_hi:[0,1,0]\n" \
  "v_fma_mix_f32 v215, v119, v175, v215 op_sel:[0,1,0] op_sel_hi:[0,1,0]\n" \
  "s_waitcnt lgkmcnt(1)\n" \
  "v_fma_mix_f32 v214, v104, v192, v214 op_sel:[0,0,0] op_sel_hi:[0,1,0]\n" \
  "v_fma_mix_f32 v215, v120, v192, v215 op_sel:[0,1,0] op_sel_hi:[0,1,0]\n" \
  "v_fma_mix_f32 v214, v105, v193, v214 op_sel:[0,0,0] op_sel_hi:[0,1,0]\n" \
  "v_fma_mix_f32 v215, v121, v193, v215 op_sel:[0,1,0] op_sel_hi:[0,1,0]\n" \
  "v_fma_mix_f32 v214, v106, v194, v214 op_sel:[0,0,0] op_sel_hi:[0,1,0]\n" \
  "v_fma_mix_f32 v215, v122, v194, v215 op_sel:[0,1,0] op_sel_hi:[0,1,0]\n" \
  "v_fma_mix_f32 v214, v107, v195, v214 op_sel:[0,0,0] op_sel_hi:[0,1,0]\n" \
  "v_fma_mix_f32 v215, v123, v195, v215 op_sel:[0,1,0] op_sel_hi:[0,1,0]\n" \
  "s_waitcnt lgkmcnt(0)\n" \
  "v_fma_mix_f32 v214, v108, v196, v214 op_sel:[0,0,0] op_sel_hi:[0,1,0]\n" \
  "v_fma_mix_f32 v215, v124, v196, v215 op_sel:[0,1,0] op_sel_hi:[0,1,0]\n" \
  "v_fma_mix_f32 v214, v109, v197, v214 op_sel:[0,0,0] op_sel_hi:[0,1,0]\n" \
  "v_fma_mix_f32 v215, v125, v197, v215 op_sel:[0,1,0] op_sel_hi:[0,1,0]\n" \
  "v_fma_mix_f32 v214, v110, v198, v214 op_sel:[0,0,0] op_sel_hi:[0,1,0]\n" \
  "v_fma_mix_f32 v215, v126, v198, v215 op_sel:[0,1,0] op_sel_hi:[0,1,0]\n" \
  "v_fma_mix_f32 v214, v111, v199, v214 op_sel:[0,0,0] op_sel_hi:[0,1,0]\n" \
  "v_fma_mix_f32 v215, v127, v199, v215 op_sel:[0,1,0] op_sel_hi:[0,1,0]\n" \
  "v_mfma_f32_32x32x4_2b_f16 v[96:127], v[228:229], v[228:229], 0\n" \
  "v_cvt_pk_bf16_f32 v220, v216, v217\n" \
  "v_cvt_pk_bf16_f32 v221, v218, v219\n" \
  "v_cvt_pk_bf16_f32 v222, v212, v213\n" \
  "v_cvt_pk_bf16_f32 v223, v214, v215\n" \
  "ds_write2_b32 v240, v220, v222 offset0:64 offset1:96\n" \
  "ds_write_b32 v240, v221 offset:1280\n" \
  "ds_write_b32 v240, v223 offset:1408\n" \
  "s_sub_u32 s92, s92, 0x200\n" \
  "s_subb_u32 s93, s93, 0\n" \
  "s_bitcmp0_b32 s67, 16\n" \
  "s_cbranch_scc1 Lend_%=\n" \
  "s_setprio 1\n" \
  "s_cmp_eq_u32 %[gen], 0\n" \
  "s_waitcnt lgkmcnt(0)\n" \
  "global_load_dwordx4 v[220:223], v233, s[92:93] offset:-512\n" \
  "s_cbranch_scc1 Lret2_%=\n" \
  "s_add_u32 s94, s92, 0x2000000\n" \
  "s_addc_u32 s95, s93, 0\n" \
  "global_load_dwordx2 v[230:231], v233, s[94:95] offset:-512\n" \
  "s_branch Lret2_%=\n" \
  "Lpc2_%=:\n" \
  "s_mov_b32 m0, s81\n" \
  "s_bitcmp0_b32 s73, 4\n" \
  "s_cselect_b32 exec_hi, -1, 0\n" \
  "global_load_lds_dwordx4 v224, s[74:75]\n" \
  "s_mov_b32 exec_hi, -1\n" \
  "s_add_u32 s81, s81, 0x400\n" \
  "s_lshr_b32 s73, s73, 1\n" \
  "s_mov_b32 s88, s80\n" \
  "v_mov_b32 v224, v225\n" \
  "v_mov_b32 v225, v226\n" \
  "v_mov_b32 v226, v227\n" \
  "s_branch Lpr2_%=\n" \
  "Lwt2_%=:\n" \
  "s_bcnt1_i32_b32 s82, s91\n" \
  "s_cmp_eq_u32 s82, 3\n" \
  "s_waitcnt vmcnt(35)\n" \
  "s_cbranch_scc1 Lwr2_%=\n" \
  "s_cmp_eq_u32 s82, 2\n" \
  "s_waitcnt vmcnt(34)\n" \
  "s_cbranch_scc1 Lwr2_%=\n" \
  "s_waitcnt vmcnt(33)\n" \
  "s_branch Lwr2_%=\n" \
  ".p2align 3\n" \
  "Lepi3_%=:\n" \
  "s_setprio 0\n" \
  "s_bitcmp1_b32 s64, 18\n" \
  "s_cbranch_scc1 Lep13_%=\n" \
  "s_cmp_eq_u32 %[gen], 0\n" \
  "s_cbranch_scc1 Lg03_%=\n" \
  "ds_read_b128 v[176:179], v232 offset:8704\n" \
  "ds_read_b128 v[180:183], v232 offset:9216\n" \
  "ds_read_b128 v[184:187], v232 offset:9728\n" \
  "ds_read_b128 v[188:191], v232 offset:10240\n" \
  "s_waitcnt lgkmcnt(3)\n" \
  "v_fma_mix_f32 v230, v64, v176, 0 op_sel:[0,0,0] op_sel_hi:[0,1,0]\n" \
  "v_fma_mix_f32 v231, v80, v176, 0 op_sel:[0,1,0] op_sel_hi:[0,1,0]\n" \
  "v_fma_mix_f32 v230, v65, v177, v230 op_sel:[0,0,0] op_sel_hi:[0,1,0]\n" \
  "v_fma_mix_f32 v231, v81, v177, v231 op_sel:[0,1,0] op_sel_hi:[0,1,0]\n" \
  "v_fma_mix_f32 v230, v66, v178, v230 op_sel:[0,0,0] op_sel_hi:[0,1,0]\n" \
  "v_fma_mix_f32 v231, v82, v178, v231 op_sel:[0,1,0] op_sel_hi:[0,1,0]\n" \
  "v_fma_mix_f32 v230, v67, v179, v230 op_sel:[0,0,0] op_sel_hi:[0,1,0]\n" \
  "v_fma_mix_f32 v231, v83, v179, v231 op_sel:[0,1,0] op_sel_hi:[0,1,0]\n" \
  "s_waitcnt lgkmcnt(2)\n" \
  "v_fma_mix_f32 v230, v68, v180, v230 op_sel:[0,0,0] op_sel_hi:[0,1,0]\n" \
  "v_fma_mix_f32 v231, v84, v180, v231 op_sel:[0,1,0] op_sel_hi:[0,1,0]\n" \
  "v_fma_mix_f32 v230, v69, v181, v230 op_sel:[0,0,0] op_sel_hi:[0,1,0]\n" \
  "v_fma_mix_f32 v231, v85, v181, v231 op_sel:[0,1,0] op_sel_hi:[0,1,0]\n" \
  "v_fma_mix_f32 v230, v70, v182, v230 op_sel:[0,0,0] op_sel_hi:[0,1,0]\n" \
  "v_fma_mix_f32 v231, v86, v182, v231 op_sel:[0,1,0] op_sel_hi:[0,1,0]\n" \
  "v_fma_mix_f32 v230, v71, v183, v230 op_sel:[0,0,0] op_sel_hi:[0,1,0]\n" \
  "v_fma_mix_f32 v231, v87, v183, v231 op_sel:[0,1,0] op_sel_hi:[0,1,0]\n" \
  "s_waitcnt lgkmcnt(1)\n" \
  "v_fma_mix_f32 v230, v72, v184, v230 op_sel:[0,0,0] op_sel_hi:[0,1,0]\n" \
  "v_fma_mix_f32 v231, v88, v184, v231 op_sel:[0,1,0] op_sel_hi:[0,1,0]\n" \
  "v_fma_mix_f32 v230, v73, v185, v230 op_sel:[0,0,0] op_sel_hi:[0,1,0]\n" \
  "v_fma_mix_f32 v231, v89, v185, v231 op_sel:[0,1,0] op_sel_hi:[0,1,0]\n" \
  "v_fma_mix_f32 v230, v74, v186, v230 op_sel:[0,0,0] op_sel_hi:[0,1,0]\n" \
  "v_fma_mix_f32 v231, v90, v186, v231 op_sel:[0,1,0] op_sel_hi:[0,1,0]\n" \
  "v_fma_mix_f32 v230, v75, v187, v230 op_sel:[0,0,0] op_sel_hi:[0,1,0]\n" \
  "v_fma_mix_f32 v231, v91, v187, v231 op_sel:[0,1,0] op_sel_hi:[0,1,0]\n" \
  "s_waitcnt lgkmcnt(0)\n" \
  "v_fma_mix_f32 v230, v76, v188, v230 op_sel:[0,0,0] op_sel_hi:[0,1,0]\n" \
  "v_fma_mix_f32 v231, v92, v188, v231 op_sel:[0,1,0] op_sel_hi:[0,1,0]\n" \
  "v_fma_mix_f32 v230, v77, v189, v230 op_sel:[0,0,0] op_sel_hi:[0,1,0]\n" \
  "v_fma_mix_f32 v231, v93, v189, v231 op_sel:[0,1,0] op_sel_hi:[0,1,0]\n" \
  "v_fma_mix_f32 v230, v78, v190, v230 op_sel:[0,0,0] op_sel_hi:[0,1,0]\n" \
  "v_fma_mix_f32 v231, v94, v190, v231 op_sel:[0,1,0] op_sel_hi:[0,1,0]\n" \
  "v_fma_mix_f32 v230, v79, v191, v230 op_sel:[0,0,0] op_sel_hi:[0,1,0]\n" \
  "v_fma_mix_f32 v231, v95, v191, v231 op_sel:[0,1,0] op_sel_hi:[0,1,0]\n" \
  "s_add_u32 s94, s92, 0x2000000\n" \
  "s_addc_u32 s95, s93, 0\n" \
  "global_store_dwordx2 v233, v[230:231], s[94:95]\n" \
  "Lg03_%=:\n" \
  "ds_read_b128 v[176:179], v233 offset:512\n" \
  "ds_read_b128 v[180:183], v233 offset:1024\n" \
  "ds_read_b128 v[184:187], v233 offset:1536\n" \
  "ds_read_b128 v[188:191], v233 offset:2048\n" \
  "ds_read_b128 v[200:203], v232 offset:4608\n" \
  "ds_read_b128 v[204:207], v232 offset:5120\n" \
  "s_waitcnt lgkmcnt(5)\n" \
  "v_fma_mix_f32 v216, v0, v176, 0 op_sel:[0,0,0] op_sel_hi:[0,1,0]\n" \
  "v_fma_mix_f32 v217, v16, v176, 0 op_sel:[0,1,0] op_sel_hi:[0,1,0]\n" \
  "v_fma_mix_f32 v216, v1, v177, v216 op_sel:[0,0,0] op_sel_hi:[0,1,0]\n" \
  "v_fma_mix_f32 v217, v17, v177, v217 op_sel:[0,1,0] op_sel_hi:[0,1,0]\n" \
  "v_fma_mix_f32 v216, v2, v178, v216 op_sel:[0,0,0] op_sel_hi:[0,1,0]\n" \
  "v_fma_mix_f32 v217, v18, v178, v217 op_sel:[0,1,0] op_sel_hi:[0,1,0]\n" \
  "v_fma_mix_f32 v216, v3, v179, v216 op_sel:[0,0,0] op_sel_hi:[0,1,0]\n" \
  "v_fma_mix_f32 v217, v19, v179, v217 op_sel:[0,1,0] op_sel_hi:[0,1,0]\n" \
  "ds_read_b128 v[176:179], v232 offset:5632\n" \
  "s_waitcnt lgkmcnt(5)\n" \
  "v_fma_mix_f32 v216, v4, v180, v216 op_sel:[0,0,0] op_sel_hi:[0,1,0]\n" \
  "v_fma_mix_f32 v217, v20, v180, v217 op_sel:[0,1,0] op_sel_hi:[0,1,0]\n" \
  "v_fma_mix_f32 v216, v5, v181, v216 op_sel:[0,0,0] op_sel_hi:[0,1,0]\n" \
  "v_fma_mix_f32 v217, v21, v181, v217 op_sel:[0,1,0] op_sel_hi:[0,1,0]\n" \
  "v_fma_mix_f32 v216, v6, v182, v216 op_sel:[0,0,0] op_sel_hi:[0,1,0]\n" \
  "v_fma_mix_f32 v217, v22, v182, v217 op_sel:[0,1,0] op_sel_hi:[0,1,0]\n" \
  "v_fma_mix_f32 v216, v7, v183, v216 op_sel:[0,0,0] op_sel_hi:[0,1,0]\n" \
  "v_fma_mix_f32 v217, v23, v183, v217 op_sel:[0,1,0] op_sel_hi:[0,1,0]\n" \
  "ds_read_b128 v[180:183], v232 offset:6144\n" \
  "s_waitcnt lgkmcnt(5)\n" \
  "v_fma_mix_f32 v216, v8, v184, v216 op_sel:[0,0,0] op_sel_hi:[0,1,0]\n" \
  "v_fma_mix_f32 v217, v24, v184, v217 op_sel:[0,1,0] op_sel_hi:[0,1,0]\n" \
  "v_fma_mix_f32 v216, v9, v185, v216 op_sel:[0,0,0] op_sel_hi:[0,1,0]\n" \
  "v_fma_mix_f32 v217, v25, v185, v217 op_sel:[0,1,0] op_sel_hi:[0,1,0]\n" \
  "v_fma_mix_f32 v216, v10, v186, v216 op_sel:[0,0,0] op_sel_hi:[0,1,0]\n" \
  "v_fma_mix_f32 v217, v26, v186, v217 op_sel:[0,1,0] op_sel_hi:[0,1,0]\n" \
  "v_fma_mix_f32 v216, v11, v187, v216 op_sel:[0,0,0] op_sel_hi:[0,1,0]\n" \
  "v_fma_mix_f32 v217, v27, v187, v217 op_sel:[0,1,0] op_sel_hi:[0,1,0]\n" \
  "ds_read_b128 v[184:187], v234 offset:2560\n" \
  "s_waitcnt lgkmcnt(5)\n" \
  "v_fma_mix_f32 v216, v12, v188, v216 op_sel:[0,0,0] op_sel_hi:[0,1,0]\n" \
  "v_fma_mix_f32 v217, v28, v188, v217 op_sel:[0,1,0] op_sel_hi:[0,1,0]\n" \
  "v_fma_mix_f32 v216, v13, v189, v216 op_sel:[0,0,0] op_sel_hi:[0,1,0]\n" \
  "v_fma_mix_f32 v217, v29, v189, v217 op_sel:[0,1,0] op_sel_hi:[0,1,0]\n" \
  "v_fma_mix_f32 v216, v14, v190, v216 op_sel:[0,0,0] op_sel_hi:[0,1,0]\n" \
  "v_fma_mix_f32 v217, v30, v190, v217 op_sel:[0,1,0] op_sel_hi:[0,1,0]\n" \
  "v_fma_mix_f32 v216, v15, v191, v216 op_sel:[0,0,0] op_sel_hi:[0,1,0]\n" \
  "v_fma_mix_f32 v217, v31, v191, v217 op_sel:[0,1,0] op_sel_hi:[0,1,0]\n" \
  "ds_read_b128 v[188:191], v234 offset:3072\n" \
  "v_mfma_f32_32x32x4_2b_f16 v[0:31], v[228:229], v[228:229], 0\n" \
  "s_waitcnt lgkmcnt(5)\n" \
  "v_fma_mix_f32 v218, v32, v200, 0 op_sel:[0,0,0] op_sel_hi:[0,1,0]\n" \
  "v_fma_mix_f32 v219, v48, v200, 0 op_sel:[0,1,0] op_sel_hi:[0,1,0]\n" \
  "v_fma_mix_f32 v218, v33, v201, v218 op_sel:[0,0,0] op_sel_hi:[0,1,0]\n" \
  "v_fma_mix_f32 v219, v49, v201, v219 op_sel:[0,1,0] op_sel_hi:[0,1,0]\n" \
  "v_fma_mix_f32 v218, v34, v202, v218 op_sel:[0,0,0] op_sel_hi:[0,1,0]\n" \
  "v_fma_mix_f32 v219, v50, v202, v219 op_sel:[0,1,0] op_sel_hi:[0,1,0]\n" \
  "v_fma_mix_f32 v218, v35, v203, v218 op_sel:[0,0,0] op_sel_hi:[0,1,0]\n" \
  "v_fma_mix_f32 v219, v51, v203, v219 op_sel:[0,1,0] op_sel_hi:[0,1,0]\n" \
  "ds_read_b128 v[200:203], v234 offset:3584\n" \
  "s_waitcnt lgkmcnt(5)\n" \
  "v_fma_mix_f32 v218, v36, v204, v218 op_sel:[0,0,0] op_sel_hi:[0,1,0]\n" \
  "v_fma_mix_f32 v219, v52, v204, v219 op_sel:[0,1,0] op_sel_hi:[0,1,0]\n" \
  "v_fma_mix_f32 v218, v37, v205, v218 op_sel:[0,0,0] op_sel_hi:[0,1,0]\n" \
  "v_fma_mix_f32 v219, v53, v205, v219 op_sel:[0,1,0] op_sel_hi:[0,1,0]\n" \
  "v_fma_mix_f32 v218, v38, v206, v218 op_sel:[0,0,0] op_sel_hi:[0,1,0]\n" \
  "v_fma_mix_f32 v219, v54, v206, v219 op_sel:[0,1,0] op_sel_hi:[0,1,0]\n" \
  "v_fma_mix_f32 v218, v39, v207, v218 op_sel:[0,0,0] op_sel_hi:[0,1,0]\n" \
  "v_fma_mix_f32 v219, v55, v207, v219 op_sel:[0,1,0] op_sel_hi:[0,1,0]\n" \
  "ds_read_b128 v[204:207], v234 offset:4096\n" \
  "s_waitcnt lgkmcnt(5)\n" \
  "v_fma_mix_f32 v218, v40, v176, v218 op_sel:[0,0,0] op_sel_hi:[0,1,0]\n" \
  "v_fma_mix_f32 v219, v56, v176, v219 op_sel:[0,1,0] op_sel_hi:[0,1,0]\n" \
  "v_fma_mix_f32 v218, v41, v177, v218 op_sel:[0,0,0] op_sel_hi:[0,1,0]\n" \
  "v_fma_mix_f32 v219, v57, v177, v219 op_sel:[0,1,0] op_sel_hi:[0,1,0]\n" \
  "v_fma_mix_f32 v218, v42, v178, v218 op_sel:[0,0,0] op_sel_hi:[0,1,0]\n" \
  "v_fma_mix_f32 v219, v58, v178, v219 op_sel:[0,1,0] op_sel_hi:[0,1,0]\n" \
  "v_fma_mix_f32 v218, v43, v179, v218 op_sel:[0,0,0] op_sel_hi:[0,1,0]\n" \
  "v_fma_mix_f32 v219, v59, v179, v219 op_sel:[0,1,0] op_sel_hi:[0,1,0]\n" \
  "ds_read_b128 v[176:179], v232 offset:8704\n" \
  "s_waitcnt lgkmcnt(5)\n" \
  "v_fma_mix_f32 v218, v44, v180, v218 op_sel:[0,0,0] op_sel_hi:[0,1,0]\n" \
  "v_fma_mix_f32 v219, v60, v180, v219 op_sel:[0,1,0] op_sel_hi:[0,1,0]\n" \
  "v_fma_mix_f32 v218, v45, v181, v218 op_sel:[0,0,0] op_sel_hi:[0,1,0]\n" \
  "v_fma_mix_f32 v219, v61, v181, v219 op_sel:[0,1,0] op_sel_hi:[0,1,0]\n" \
  "v_fma_mix_f32 v218, v46, v182, v218 op_sel:[0,0,0] op_sel_hi:[0,1,0]\n" \
  "v_fma_mix_f32 v219, v62, v182, v219 op_sel:[0,1,0] op_sel_hi:[0,1,0]\n" \
  "v_fma_mix_f32 v218, v47, v183, v218 op_sel:[0,0,0] op_sel_hi:[0,1,0]\n" \
  "v_fma_mix_f32 v219, v63, v183, v219 op_sel:[0,1,0] op_sel_hi:[0,1,0]\n" \
  "ds_read_b128 v[180:183], v232 offset:9216\n" \
  "v_mfma_f32_32x32x4_2b_f16 v[32:63], v[228:229], v[228:229], 0\n" \
  "s_waitcnt lgkmcnt(5)\n" \
  "v_fma_mix_f32 v208, v64, v184, 0 op_sel:[0,0,0] op_sel_hi:[0,1,0]\n" \
  "v_fma_mix_f32 v209, v80, v184, 0 op_sel:[0,1,0] op_sel_hi:[0,1,0]\n" \
  "v_fma_mix_f32 v208, v65, v185, v208 op_sel:[0,0,0] op_sel_hi:[0,1,0]\n" \
  "v_fma_mix_f32 v209, v81, v185, v209 op_sel:[0,1,0] op_sel_hi:[0,1,0]\n" \
  "v_fma_mix_f32 v208, v66, v186, v208 op_sel:[0,0,0] op_sel_hi:[0,1,0]\n" \
  "v_fma_mix_f32 v209, v82, v186, v209 op_sel:[0,1,0] op_sel_hi:[0,1,0]\n" \
  "v_fma_mix_f32 v208, v67, v187, v208 op_sel:[0,0,0] op_sel_hi:[0,1,0]\n" \
  "v_fma_mix_f32 v209, v83, v187, v209 op_sel:[0,1,0] op_sel_hi:[0,1,0]\n" \
  "ds_read_b128 v[184:187], v232 offset:9728\n" \
  "s_waitcnt lgkmcnt(5)\n" \
  "v_fma_mix_f32 v208, v68, v188, v208 op_sel:[0,0,0] op_sel_hi:[0,1,0]\n" \
  "v_fma_mix_f32 v209, v84, v188, v209 op_sel:[0,1,0] op_sel_hi:[0,1,0]\n" \
  "v_fma_mix_f32 v208, v69, v189, v208 op_sel:[0,0,0] op_sel_hi:[0,1,0]\n" \
  "v_fma_mix_f32 v209, v85, v189, v209 op_sel:[0,1,0] op_sel_hi:[0,1,0]\n" \
  "v_fma_mix_f32 v208, v70, v190, v208 op_sel:[0,0,0] op_sel_hi:[0,1,0]\n" \
  "v_fma_mix_f32 v209, v86, v190, v209 op_sel:[0,1,0] op_sel_hi:[0,1,0]\n" \
  "v_fma_mix_f32 v208, v71, v191, v208 op_sel:[0,0,0] op_sel_hi:[0,1,0]\n" \
  "v_fma_mix_f32 v209, v87, v191, v209 op_sel:[0,1,0] op_sel_hi:[0,1,0]\n" \
  "ds_read_b128 v[188:191], v232 offset:10240\n" \
  "s_waitcnt lgkmcnt(5)\n" \
  "v_fma_mix_f32 v208, v72, v200, v208 op_sel:[0,0,0] op_sel_hi:[0,1,0]\n" \
  "v_fma_mix_f32 v209, v88, v200, v209 op_sel:[0,1,0] op_sel_hi:[0,1,0]\n" \
  "v_fma_mix_f32 v208, v73, v201, v208 op_sel:[0,0,0] op_sel_hi:[0,1,0]\n" \
  "v_fma_mix_f32 v209, v89, v201, v209 op_sel:[0,1,0] op_sel_hi:[0,1,0]\n" \
  "v_fma_mix_f32 v208, v74, v202, v208 op_sel:[0,0,0] op_sel_hi:[0,1,0]\n" \
  "v_fma_mix_f32 v209, v90, v202, v209 op_sel:[0,1,0] op_sel_hi:[0,1,0]\n" \
  "v_fma_mix_f32 v208, v75, v203, v208 op_sel:[0,0,0] op_sel_hi:[0,1,0]\n" \
  "v_fma_mix_f32 v209, v91, v203, v209 op_sel:[0,1,0] op_sel_hi:[0,1,0]\n" \
  "s_waitcnt lgkmcnt(4)\n" \
  "v_fma_mix_f32 v208, v76, v204, v208 op_sel:[0,0,0] op_sel_hi:[0,1,0]\n" \
  "v_fma_mix_f32 v209, v92, v204, v209 op_sel:[0,1,0] op_sel_hi:[0,1,0]\n" \
  "v_fma_mix_f32 v208, v77, v205, v208 op_sel:[0,0,0] op_sel_hi:[0,1,0]\n" \
  "v_fma_mix_f32 v209, v93, v205, v209 op_sel:[0,1,0] op_sel_hi:[0,1,0]\n" \
  "v_fma_mix_f32 v208, v78, v206, v208 op_sel:[0,0,0] op_sel_hi:[0,1,0]\n" \
  "v_fma_mix_f32 v209, v94, v206, v209 op_sel:[0,1,0] op_sel_hi:[0,1,0]\n" \
  "v_fma_mix_f32 v208, v79, v207, v208 op_sel:[0,0,0] op_sel_hi:[0,1,0]\n" \
  "v_fma_mix_f32 v209, v95, v207, v209 op_sel:[0,1,0] op_sel_hi:[0,1,0]\n" \
  "s_waitcnt lgkmcnt(3)\n" \
  "v_fma_mix_f32 v210, v96, v176, 0 op_sel:[0,0,0] op_sel_hi:[0,1,0]\n" \
  "v_fma_mix_f32 v211, v112, v176, 0 op_sel:[0,1,0] op_sel_hi:[0,1,0]\n" \
  "v_fma_mix_f32 v210, v97, v177, v210 op_sel:[0,0,0] op_sel_hi:[0,1,0]\n" \
  "v_fma_mix_f32 v211, v113, v177, v211 op_sel:[0,1,0] op_sel_hi:[0,1,0]\n" \
  "v_fma_mix_f32 v210, v98, v178, v210 op_sel:[0,0,0] op_sel_hi:[0,1,0]\n" \
  "v_fma_mix_f32 v211, v114, v178, v211 op_sel:[0,1,0] op_sel_hi:[0,1,0]\n" \
  "v_fma_mix_f32 v210, v99, v179, v210 op_sel:[0,0,0] op_sel_hi:[0,1,0]\n" \
  "v_fma_mix_f32 v211, v115, v179, v211 op_sel:[0,1,0] op_sel_hi:[0,1,0]\n" \
  "s_waitcnt lgkmcnt(2)\n" \
  "v_fma_mix_f32 v210, v100, v180, v210 op_sel:[0,0,0] op_sel_hi:[0,1,0]\n" \
  "v_fma_mix_f32 v211, v116, v180, v211 op_sel:[0,1,0] op_sel_hi:[0,1,0]\n" \
  "v_fma_mix_f32 v210, v101, v181, v210 op_sel:[0,0,0] op_sel_hi:[0,1,0]\n" \
  "v_fma_mix_f32 v211, v117, v181, v211 op_sel:[0,1,0] op_sel_hi:[0,1,0]\n" \
  "v_fma_mix_f32 v210, v102, v182, v210 op_sel:[0,0,0] op_sel_hi:[0,1,0]\n" \
  "v_fma_mix_f32 v211, v118, v182, v211 op_sel:[0,1,0] op_sel_hi:[0,1,0]\n" \
  "v_fma_mix_f32 v210, v103, v183, v210 op_sel:[0,0,0] op_sel_hi:[0,1,0]\n" \
  "v_fma_mix_f32 v211, v119, v183, v211 op_sel:[0,1,0] op_sel_hi:[0,1,0]\n" \
  "s_waitcnt lgkmcnt(1)\n" \
  "v_fma_mix_f32 v210, v104, v184, v210 op_sel:[0,0,0] op_sel_hi:[0,1,0]\n" \
  "v_fma_mix_f32 v211, v120, v184, v211 op_sel:[0,1,0] op_sel_hi:[0,1,0]\n" \
  "v_fma_mix_f32 v210, v105, v185, v210 op_sel:[0,0,0] op_sel_hi:[0,1,0]\n" \
  "v_fma_mix_f32 v211, v121, v185, v211 op_sel:[0,1,0] op_sel_hi:[0,1,0]\n" \
  "v_fma_mix_f32 v210, v106, v186, v210 op_sel:[0,0,0] op_sel_hi:[0,1,0]\n" \
  "v_fma_mix_f32 v211, v122, v186, v211 op_sel:[0,1,0] op_sel_hi:[0,1,0]\n" \
  "v_fma_mix_f32 v210, v107, v187, v210 op_sel:[0,0,0] op_sel_hi:[0,1,0]\n" \
  "v_fma_mix_f32 v211, v123, v187, v211 op_sel:[0,1,0] op_sel_hi:[0,1,0]\n" \
  "s_waitcnt lgkmcnt(0)\n" \
  "v_fma_mix_f32 v210, v108, v188, v210 op_sel:[0,0,0] op_sel_hi:[0,1,0]\n" \
  "v_fma_mix_f32 v211, v124, v188, v211 op_sel:[0,1,0] op_sel_hi:[0,1,0]\n" \
  "v_fma_mix_f32 v210, v109, v189, v210 op_sel:[0,0,0] op_sel_hi:[0,1,0]\n" \
  "v_fma_mix_f32 v211, v125, v189, v211 op_sel:[0,1,0] op_sel_hi:[0,1,0]\n" \
  "v_fma_mix_f32 v210, v110, v190, v210 op_sel:[0,0,0] op_sel_hi:[0,1,0]\n" \
  "v_fma_mix_f32 v211, v126, v190, v211 op_sel:[0,1,0] op_sel_hi:[0,1,0]\n" \
  "v_fma_mix_f32 v210, v111, v191, v210 op_sel:[0,0,0] op_sel_hi:[0,1,0]\n" \
  "v_fma_mix_f32 v211, v127, v191, v211 op_sel:[0,1,0] op_sel_hi:[0,1,0]\n" \
  "v_cvt_pk_bf16_f32 v220, v216, v217\n" \
  "v_cvt_pk_bf16_f32 v221, v218, v219\n" \
  "v_cvt_pk_bf16_f32 v222, v208, v209\n" \
  "v_cvt_pk_bf16_f32 v223, v210, v211\n" \
  "s_getpc_b64 s[70:71]\n" \
  "s_add_u32 s70, s70, 12\n" \
  "s_addc_u32 s71, s71, 0\n" \
  "s_branch Lasub_%=\n" \
  "v_mad_u32_u16 v100, s64, 64, %[c_a]\n" \
  "v_mad_u32_u16 v101, s64, 20, %[c_y]\n" \
  "ds_read_b64_tr_b16 v[216:217], v100\n" \
  "ds_read_b64_tr_b16 v[218:219], v100 offset:128\n" \
  "ds_read_b128 v[192:195], v101\n" \
  "ds_read_b128 v[196:199], v101 offset:32\n" \
  "global_store_dwordx4 v233, v[220:223], s[92:93] sc1\n" \
  "s_add_u32 s92, s92, 0x200\n" \
  "s_addc_u32 s93, s93, 0\n" \
  "v_mfma_f32_32x32x4_2b_f16 v[64:95], v[228:229], v[228:229], 0\n" \
  "v_mfma_f32_32x32x4_2b_f16 v[96:127], v[228:229], v[228:229], 0\n" \
  "s_waitcnt lgkmcnt(0)\n" \
  "v_pk_mul_f16 v192, v192, v216\n" \
  "v_pk_mul_f16 v193, v193, v217\n" \
  "v_pk_mul_f16 v194, v194, v218\n" \
  "v_pk_mul_f16 v195, v195, v219\n" \
  "v_pk_mul_f16 v196, v196, v216\n" \
  "v_pk_mul_f16 v197, v197, v217\n" \
  "v_pk_mul_f16 v198, v198, v218\n" \
  "v_pk_mul_f16 v199, v199, v219\n" \
  "s_setprio 1\n" \
  "s_branch Lret3_%=\n" \
  "Lep13_%=:\n" \
  "s_lshr_b32 s82, s64, 15\n" \
  "s_andn2_b32 s82, s82, 15\n" \
  "v_xad_u32 v240, s82, %[c_x], %[c_u]\n" \
  "s_waitcnt vmcnt(32)\n" \
  "ds_write2_b32 v240, v220, v222 offset1:32\n" \
  "ds_write_b32 v240, v221 offset:1024\n" \
  "ds_write_b32 v240, v223 offset:1152\n" \
  "s_cmp_eq_u32 %[gen], 0\n" \
  "s_cbranch_scc1 Lg13_%=\n" \
  "s_nop 7\n" \
  "ds_read_b128 v[176:179], v232 offset:8704\n" \
  "ds_read_b128 v[180:183], v232 offset:9216\n" \
  "ds_read_b128 v[184:187], v232 offset:9728\n" \
  "ds_read_b128 v[188:191], v232 offset:10240\n" \
  "s_waitcnt lgkmcnt(3)\n" \
  "v_fma_mix_f32 v216, v32, v176, 0 op_sel:[0,0,0] op_sel_hi:[0,1,0]\n" \
  "v_fma_mix_f32 v217, v48, v176, 0 op_sel:[0,1,0] op_sel_hi:[0,1,0]\n" \
  "v_fma_mix_f32 v218, v96, v176, 0 op_sel:[0,0,0] op_sel_hi:[0,1,0]\n" \
  "v_fma_mix_f32 v219, v112, v176, 0 op_sel:[0,1,0] op_sel_hi:[0,1,0]\n" \
  "v_fma_mix_f32 v216, v33, v177, v216 op_sel:[0,0,0] op_sel_hi:[0,1,0]\n" \
  "v_fma_mix_f32 v217, v49, v177, v217 op_sel:[0,1,0] op_sel_hi:[0,1,0]\n" \
  "v_fma_mix_f32 v218, v97, v177, v218 op_sel:[0,0,0] op_sel_hi:[0,1,0]\n" \
  "v_fma_mix_f32 v219, v113, v177, v219 op_sel:[0,1,0] op_sel_hi:[0,1,0]\n" \
  "v_fma_mix_f32 v216, v34, v178, v216 op_sel:[0,0,0] op_sel_hi:[0,1,0]\n" \
  "v_fma_mix_f32 v217, v50, v178, v217 op_sel:[0,1,0] op_sel_hi:[0,1,0]\n" \
  "v_fma_mix_f32 v218, v98, v178, v218 op_sel:[0,0,0] op_sel_hi:[0,1,0]\n" \
  "v_fma_mix_f32 v219, v114, v178, v219 op_sel:[0,1,0] op_sel_hi:[0,1,0]\n" \
  "v_fma_mix_f32 v216, v35, v179, v216 op_sel:[0,0,0] op_sel_hi:[0,1,0]\n" \
  "v_fma_mix_f32 v217, v51, v179, v217 op_sel:[0,1,0] op_sel_hi:[0,1,0]\n" \
  "v_fma_mix_f32 v218, v99, v179, v218 op_sel:[0,0,0] op_sel_hi:[0,1,0]\n" \
  "v_fma_mix_f32 v219, v115, v179, v219 op_sel:[0,1,0] op_sel_hi:[0,1,0]\n" \
  "s_waitcnt lgkmcnt(2)\n" \
  "v_fma_mix_f32 v216, v36, v180, v216 op_sel:[0,0,0] op_sel_hi:[0,1,0]\n" \
  "v_fma_mix_f32 v217, v52, v180, v217 op_sel:[0,1,0] op_sel_hi:[0,1,0]\n" \
  "v_fma_mix_f32 v218, v100, v180, v218 op_sel:[0,0,0] op_sel_hi:[0,1,0]\n" \
  "v_fma_mix_f32 v219, v116, v180, v219 op_sel:[0,1,0] op_sel_hi:[0,1,0]\n" \
  "v_fma_mix_f32 v216, v37, v181, v216 op_sel:[0,0,0] op_sel_hi:[0,1,0]\n" \
  "v_fma_mix_f32 v217, v53, v181, v217 op_sel:[0,1,0] op_sel_hi:[0,1,0]\n" \
  "v_fma_mix_f32 v218, v101, v181, v218 op_sel:[0,0,0] op_sel_hi:[0,1,0]\n" \
  "v_fma_mix_f32 v219, v117, v181, v219 op_sel:[0,1,0] op_sel_hi:[0,1,0]\n" \
  "v_fma_mix_f32 v216, v38, v182, v216 op_sel:[0,0,0] op_sel_hi:[0,1,0]\n" \
  "v_fma_mix_f32 v217, v54, v182, v217 op_sel:[0,1,0] op_sel_hi:[0,1,0]\n" \
  "v_fma_mix_f32 v218, v102, v182, v218 op_sel:[0,0,0] op_sel_hi:[0,1,0]\n" \
  "v_fma_mix_f32 v219, v118, v182, v219 op_sel:[0,1,0] op_sel_hi:[0,1,0]\n" \
  "v_fma_mix_f32 v216, v39, v183, v216 op_sel:[0,0,0] op_sel_hi:[0,1,0]\n" \
  "v_fma_mix_f32 v217, v55, v183, v217 op_sel:[0,1,0] op_sel_hi:[0,1,0]\n" \
  "v_fma_mix_f32 v218, v103, v183, v218 op_sel:[0,0,0] op_sel_hi:[0,1,0]\n" \
  "v_fma_mix_f32 v219, v119, v183, v219 op_sel:[0,1,0] op_sel_hi:[0,1,0]\n" \
  "s_waitcnt lgkmcnt(1)\n" \
  "v_fma_mix_f32 v216, v40, v184, v216 op_sel:[0,0,0] op_sel_hi:[0,1,0]\n" \
  "v_fma_mix_f32 v217, v56, v184, v217 op_sel:[0,1,0] op_sel_hi:[0,1,0]\n" \
  "v_fma_mix_f32 v218, v104, v184, v218 op_sel:[0,0,0] op_sel_hi:[0,1,0]\n" \
  "v_fma_mix_f32 v219, v120, v184, v219 op_sel:[0,1,0] op_sel_hi:[0,1,0]\n" \
  "v_fma_mix_f32 v216, v41, v185, v216 op_sel:[0,0,0] op_sel_hi:[0,1,0]\n" \
  "v_fma_mix_f32 v217, v57, v185, v217 op_sel:[0,1,0] op_sel_hi:[0,1,0]\n" \
  "v_fma_mix_f32 v218, v105, v185, v218 op_sel:[0,0,0] op_sel_hi:[0,1,0]\n" \
  "v_fma_mix_f32 v219, v121, v185, v219 op_sel:[0,1,0] op_sel_hi:[0,1,0]\n" \
  "v_fma_mix_f32 v216, v42, v186, v216 op_sel:[0,0,0] op_sel_hi:[0,1,0]\n" \
  "v_fma_mix_f32 v217, v58, v186, v217 op_sel:[0,1,0] op_sel_hi:[0,1,0]\n" \
  "v_fma_mix_f32 v218, v106, v186, v218 op_sel:[0,0,0] op_sel_hi:[0,1,0]\n" \
  "v_fma_mix_f32 v219, v122, v186, v219 op_sel:[0,1,0] op_sel_hi:[0,1,0]\n" \
  "v_fma_mix_f32 v216, v43, v187, v216 op_sel:[0,0,0] op_sel_hi:[0,1,0]\n" \
  "v_fma_mix_f32 v217, v59, v187, v217 op_sel:[0,1,0] op_sel_hi:[0,1,0]\n" \
  "v_fma_mix_f32 v218, v107, v187, v218 op_sel:[0,0,0] op_sel_hi:[0,1,0]\n" \
  "v_fma_mix_f32 v219, v123, v187, v219 op_sel:[0,1,0] op_sel_hi:[0,1,0]\n" \
  "s_waitcnt lgkmcnt(0)\n" \
  "v_fma_mix_f32 v216, v44, v188, v216 op_sel:[0,0,0] op_sel_hi:[0,1,0]\n" \
  "v_fma_mix_f32 v217, v60, v188, v217 op_sel:[0,1,0] op_sel_hi:[0,1,0]\n" \
  "v_fma_mix_f32 v218, v108, v188, v218 op_sel:[0,0,0] op_sel_hi:[0,1,0]\n" \
  "v_fma_mix_f32 v219, v124, v188, v219 op_sel:[0,1,0] op_sel_hi:[0,1,0]\n" \
  "v_fma_mix_f32 v216, v45, v189, v216 op_sel:[0,0,0] op_sel_hi:[0,1,0]\n" \
  "v_fma_mix_f32 v217, v61, v189, v217 op_sel:[0,1,0] op_sel_hi:[0,1,0]\n" \
  "v_fma_mix_f32 v218, v109, v189, v218 op_sel:[0,0,0] op_sel_hi:[0,1,0]\n" \
  "v_fma_mix_f32 v219, v125, v189, v219 op_sel:[0,1,0] op_sel_hi:[0,1,0]\n" \
  "v_fma_mix_f32 v216, v46, v190, v216 op_sel:[0,0,0] op_sel_hi:[0,1,0]\n" \
  "v_fma_mix_f32 v217, v62, v190, v217 op_sel:[0,1,0] op_sel_hi:[0,1,0]\n" \
  "v_fma_mix_f32 v218, v110, v190, v218 op_sel:[0,0,0] op_sel_hi:[0,1,0]\n" \
  "v_fma_mix_f32 v219, v126, v190, v219 op_sel:[0,1,0] op_sel_hi:[0,1,0]\n" \
  "v_fma_mix_f32 v216, v47, v191, v216 op_sel:[0,0,0] op_sel_hi:[0,1,0]\n" \
  "v_fma_mix_f32 v217, v63, v191, v217 op_sel:[0,1,0] op_sel_hi:[0,1,0]\n" \
  "v_fma_mix_f32 v218, v111, v191, v218 op_sel:[0,0,0] op_sel_hi:[0,1,0]\n" \
  "v_fma_mix_f32 v219, v127, v191, v219 op_sel:[0,1,0] op_sel_hi:[0,1,0]\n" \
  "v_cvt_pk_bf16_f32 v216, v216, v217\n" \
  "v_cvt_pk_bf16_f32 v218, v218, v219\n" \
  "v_cvt_pk_bf16_f32 v217, v230, v231\n" \
  "s_lshr_b32 s83, s64, 24\n" \
  "s_mul_i32 s83, s83, 160\n" \
  "s_and_b32 s82, s82, 0x70\n" \
  "v_xor_b32 v219, s82, %[c_x]\n" \
  "v_add_u32 v219, s83, v219\n" \
  "s_mov_b32 exec_hi, 0\n" \
  "ds_write_b32 v219, v216 offset:%[l_ys]\n" \
  "s_mov_b32 exec_hi, -1\n" \
  "s_mov_b32 exec_lo, 0\n" \
  "ds_write_b32 v219, v217 offset:%[l_ys]+128\n" \
  "ds_write_b32 v219, v218 offset:%[l_ys]+256\n" \
  "s_mov_b32 exec_lo, -1\n" \
  "s_waitcnt lgkmcnt(0)\n" \
  "Lg13_%=:\n" \
  "ds_read_b128 v[176:179], v236 offset:2560\n" \
  "ds_read_b128 v[180:183], v236 offset:3072\n" \
  "ds_read_b128 v[184:187], v236 offset:3584\n" \
  "ds_read_b128 v[188:191], v236 offset:4096\n" \
  "ds_read_b128 v[200:203], v235 offset:6656\n" \
  "ds_read_b128 v[204:207], v235 offset:7168\n" \
  "s_waitcnt lgkmcnt(5)\n" \
  "v_fma_mix_f32 v218, v32, v176, 0 op_sel:[0,0,0] op_sel_hi:[0,1,0]\n" \
  "v_fma_mix_f32 v219, v48, v176, 0 op_sel:[0,1,0] op_sel_hi:[0,1,0]\n" \
  "v_fma_mix_f32 v218, v33, v177, v218 op_sel:[0,0,0] op_sel_hi:[0,1,0]\n" \
  "v_fma_mix_f32 v219, v49, v177, v219 op_sel:[0,1,0] op_sel_hi:[0,1,0]\n" \
  "v_fma_mix_f32 v218, v34, v178, v218 op_sel:[0,0,0] op_sel_hi:[0,1,0]\n" \
  "v_fma_mix_f32 v219, v50, v178, v219 op_sel:[0,1,0] op_sel_hi:[0,1,0]\n" \
  "v_fma_mix_f32 v218, v35, v179, v218 op_sel:[0,0,0] op_sel_hi:[0,1,0]\n" \
  "v_fma_mix_f32 v219, v51, v179, v219 op_sel:[0,1,0] op_sel_hi:[0,1,0]\n" \
  "ds_read_b128 v[176:179], v235 offset:7680\n" \
  "s_waitcnt lgkmcnt(5)\n" \
  "v_fma_mix_f32 v218, v36, v180, v218 op_sel:[0,0,0] op_sel_hi:[0,1,0]\n" \
  "v_fma_mix_f32 v219, v52, v180, v219 op_sel:[0,1,0] op_sel_hi:[0,1,0]\n" \
  "v_fma_mix_f32 v218, v37, v181, v218 op_sel:[0,0,0] op_sel_hi:[0,1,0]\n" \
  "v_fma_mix_f32 v219, v53, v181, v219 op_sel:[0,1,0] op_sel_hi:[0,1,0]\n" \
  "v_fma_mix_f32 v218, v38, v182, v218 op_sel:[0,0,0] op_sel_hi:[0,1,0]\n" \
  "v_fma_mix_f32 v219, v54, v182, v219 op_sel:[0,1,0] op_sel_hi:[0,1,0]\n" \
  "v_fma_mix_f32 v218, v39, v183, v218 op_sel:[0,0,0] op_sel_hi:[0,1,0]\n" \
  "v_fma_mix_f32 v219, v55, v183, v219 op_sel:[0,1,0] op_sel_hi:[0,1,0]\n" \
  "ds_read_b128 v[180:183], v235 offset:8192\n" \
  "s_waitcnt lgkmcnt(5)\n" \
  "v_fma_mix_f32 v218, v40, v184, v218 op_sel:[0,0,0] op_sel_hi:[0,1,0]\n" \
  "v_fma_mix_f32 v219, v56, v184, v219 op_sel:[0,1,0] op_sel_hi:[0,1,0]\n" \
  "v_fma_mix_f32 v218, v41, v185, v218 op_sel:[0,0,0] op_sel_hi:[0,1,0]\n" \
  "v_fma_mix_f32 v219, v57, v185, v219 op_sel:[0,1,0] op_sel_hi:[0,1,0]\n" \
  "v_fma_mix_f32 v218, v42, v186, v218 op_sel:[0,0,0] op_sel_hi:[0,1,0]\n" \
  "v_fma_mix_f32 v219, v58, v186, v219 op_sel:[0,1,0] op_sel_hi:[0,1,0]\n" \
  "v_fma_mix_f32 v218, v43, v187, v218 op_sel:[0,0,0] op_sel_hi:[0,1,0]\n" \
  "v_fma_mix_f32 v219, v59, v187, v219 op_sel:[0,1,0] op_sel_hi:[0,1,0]\n" \
  "ds_read_b128 v[184:187], v237 offset:2560\n" \
  "s_waitcnt lgkmcnt(5)\n" \
  "v_fma_mix_f32 v218, v44, v188, v218 op_sel:[0,0,0] op_sel_hi:[0,1,0]\n" \
  "v_fma_mix_f32 v219, v60, v188, v219 op_sel:[0,1,0] op_sel_hi:[0,1,0]\n" \
  "v_fma_mix_f32 v218, v45, v189, v218 op_sel:[0,0,0] op_sel_hi:[0,1,0]\n" \
  "v_fma_mix_f32 v219, v61, v189, v219 op_sel:[0,1,0] op_sel_hi:[0,1,0]\n" \
  "v_fma_mix_f32 v218, v46, v190, v218 op_sel:[0,0,0] op_sel_hi:[0,1,0]\n" \
  "v_fma_mix_f32 v219, v62, v190, v219 op_sel:[0,1,0] op_sel_hi:[0,1,0]\n" \
  "v_fma_mix_f32 v218, v47, v191, v218 op_sel:[0,0,0] op_sel_hi:[0,1,0]\n" \
  "v_fma_mix_f32 v219, v63, v191, v219 op_sel:[0,1,0] op_sel_hi:[0,1,0]\n" \
  "ds_read_b128 v[188:191], v237 offset:3072\n" \
  "v_mfma_f32_32x32x4_2b_f16 v[32:63], v[228:229], v[228:229], 0\n" \
  "s_waitcnt lgkmcnt(5)\n" \
  "v_fma_mix_f32 v216, v0, v200, 0 op_sel:[0,0,0] op_sel_hi:[0,1,0]\n" \
  "v_fma_mix_f32 v217, v16, v200, 0 op_sel:[0,1,0] op_sel_hi:[0,1,0]\n" \
  "v_fma_mix_f32 v208, v64, v200, 0 op_sel:[0,0,0] op_sel_hi:[0,1,0]\n" \
  "v_fma_mix_f32 v209, v80, v200, 0 op_sel:[0,1,0] op_sel_hi:[0,1,0]\n" \
  "v_fma_mix_f32 v216, v1, v201, v216 op_sel:[0,0,0] op_sel_hi:[0,1,0]\n" \
  "v_fma_mix_f32 v217, v17, v201, v217 op_sel:[0,1,0] op_sel_hi:[0,1,0]\n" \
  "v_fma_mix_f32 v208, v65, v201, v208 op_sel:[0,0,0] op_sel_hi:[0,1,0]\n" \
  "v_fma_mix_f32 v209, v81, v201, v209 op_sel:[0,1,0] op_sel_hi:[0,1,0]\n" \
  "v_fma_mix_f32 v216, v2, v202, v216 op_sel:[0,0,0] op_sel_hi:[0,1,0]\n" \
  "v_fma_mix_f32 v217, v18, v202, v217 op_sel:[0,1,0] op_sel_hi:[0,1,0]\n" \
  "v_fma_mix_f32 v208, v66, v202, v208 op_sel:[0,0,0] op_sel_hi:[0,1,0]\n" \
  "v_fma_mix_f32 v209, v82, v202, v209 op_sel:[0,1,0] op_sel_hi:[0,1,0]\n" \
  "v_fma_mix_f32 v216, v3, v203, v216 op_sel:[0,0,0] op_sel_hi:[0,1,0]\n" \
  "v_fma_mix_f32 v217, v19, v203, v217 op_sel:[0,1,0] op_sel_hi:[0,1,0]\n" \
  "v_fma_mix_f32 v208, v67, v203, v208 op_sel:[0,0,0] op_sel_hi:[0,1,0]\n" \
  "v_fma_mix_f32 v209, v83, v203, v209 op_sel:[0,1,0] op_sel_hi:[0,1,0]\n" \
  "ds_read_b128 v[200:203], v237 offset:3584\n" \
  "s_waitcnt lgkmcnt(5)\n" \
  "v_fma_mix_f32 v216, v4, v204, v216 op_sel:[0,0,0] op_sel_hi:[0,1,0]\n" \
  "v_fma_mix_f32 v217, v20, v204, v217 op_sel:[0,1,0] op_sel_hi:[0,1,0]\n" \
  "v_fma_mix_f32 v208, v68, v204, v208 op_sel:[0,0,0] op_sel_hi:[0,1,0]\n" \
  "v_fma_mix_f32 v209, v84, v204, v209 op_sel:[0,1,0] op_sel_hi:[0,1,0]\n" \
  "v_fma_mix_f32 v216, v5, v205, v216 op_sel:[0,0,0] op_sel_hi:[0,1,0]\n" \
  "v_fma_mix_f32 v217, v21, v205, v217 op_sel:[0,1,0] op_sel_hi:[0,1,0]\n" \
  "v_fma_mix_f32 v208, v69, v205, v208 op_sel:[0,0,0] op_sel_hi:[0,1,0]\n" \
  "v_fma_mix_f32 v209, v85, v205, v209 op_sel:[0,1,0] op_sel_hi:[0,1,0]\n" \
  "v_fma_mix_f32 v216, v6, v206, v216 op_sel:[0,0,0] op_sel_hi:[0,1,0]\n" \
  "v_fma_mix_f32 v217, v22, v206, v217 op_sel:[0,1,0] op_sel_hi:[0,1,0]\n" \
  "v_fma_mix_f32 v208, v70, v206, v208 op_sel:[0,0,0] op_sel_hi:[0,1,0]\n" \
  "v_fma_mix_f32 v209, v86, v206, v209 op_sel:[0,1,0] op_sel_hi:[0,1,0]\n" \
  "v_fma_mix_f32 v216, v7, v207, v216 op_sel:[0,0,0] op_sel_hi:[0,1,0]\n" \
  "v_fma_mix_f32 v217, v23, v207, v217 op_sel:[0,1,0] op_sel_hi:[0,1,0]\n" \
  "v_fma_mix_f32 v208, v71, v207, v208 op_sel:[0,0,0] op_sel_hi:[0,1,0]\n" \
  "v_fma_mix_f32 v209, v87, v207, v209 op_sel:[0,1,0] op_sel_hi:[0,1,0]\n" \
  "ds_read_b128 v[204:207], v237 offset:4096\n" \
  "s_waitcnt lgkmcnt(5)\n" \
  "v_fma_mix_f32 v216, v8, v176, v216 op_sel:[0,0,0] op_sel_hi:[0,1,0]\n" \
  "v_fma_mix_f32 v217, v24, v176, v217 op_sel:[0,1,0] op_sel_hi:[0,1,0]\n" \
  "v_fma_mix_f32 v208, v72, v176, v208 op_sel:[0,0,0] op_sel_hi:[0,1,0]\n" \
  "v_fma_mix_f32 v209, v88, v176, v209 op_sel:[0,1,0] op_sel_hi:[0,1,0]\n" \
  "v_fma_mix_f32 v216, v9, v177, v216 op_sel:[0,0,0] op_sel_hi:[0,1,0]\n" \
  "v_fma_mix_f32 v217, v25, v177, v217 op_sel:[0,1,0] op_sel_hi:[0,1,0]\n" \
  "v_fma_mix_f32 v208, v73, v177, v208 op_sel:[0,0,0] op_sel_hi:[0,1,0]\n" \
  "v_fma_mix_f32 v209, v89, v177, v209 op_sel:[0,1,0] op_sel_hi:[0,1,0]\n" \
  "v_fma_mix_f32 v216, v10, v178, v216 op_sel:[0,0,0] op_sel_hi:[0,1,0]\n" \
  "v_fma_mix_f32 v217, v26, v178, v217 op_sel:[0,1,0] op_sel_hi:[0,1,0]\n" \
  "v_fma_mix_f32 v208, v74, v178, v208 op_sel:[0,0,0] op_sel_hi:[0,1,0]\n" \
  "v_fma_mix_f32 v209, v90, v178, v209 op_sel:[0,1,0] op_sel_hi:[0,1,0]\n" \
  "v_fma_mix_f32 v216, v11, v179, v216 op_sel:[0,0,0] op_sel_hi:[0,1,0]\n" \
  "v_fma_mix_f32 v217, v27, v179, v217 op_sel:[0,1,0] op_sel_hi:[0,1,0]\n" \
  "v_fma_mix_f32 v208, v75, v179, v208 op_sel:[0,0,0] op_sel_hi:[0,1,0]\n" \
  "v_fma_mix_f32 v209, v91, v179, v209 op_sel:[0,1,0] op_sel_hi:[0,1,0]\n" \
  "s_waitcnt lgkmcnt(4)\n" \
  "v_fma_mix_f32 v216, v12, v180, v216 op_sel:[0,0,0] op_sel_hi:[0,1,0]\n" \
  "v_fma_mix_f32 v217, v28, v180, v217 op_sel:[0,1,0] op_sel_hi:[0,1,0]\n" \
  "v_fma_mix_f32 v208, v76, v180, v208 op_sel:[0,0,0] op_sel_hi:[0,1,0]\n" \
  "v_fma_mix_f32 v209, v92, v180, v209 op_sel:[0,1,0] op_sel_hi:[0,1,0]\n" \
  "v_fma_mix_f32 v216, v13, v181, v216 op_sel:[0,0,0] op_sel_hi:[0,1,0]\n" \
  "v_fma_mix_f32 v217, v29, v181, v217 op_sel:[0,1,0] op_sel_hi:[0,1,0]\n" \
  "v_fma_mix_f32 v208, v77, v181, v208 op_sel:[0,0,0] op_sel_hi:[0,1,0]\n" \
  "v_fma_mix_f32 v209, v93, v181, v209 op_sel:[0,1,0] op_sel_hi:[0,1,0]\n" \
  "v_fma_mix_f32 v216, v14, v182, v216 op_sel:[0,0,0] op_sel_hi:[0,1,0]\n" \
  "v_fma_mix_f32 v217, v30, v182, v217 op_sel:[0,1,0] op_sel_hi:[0,1,0]\n" \
  "v_fma_mix_f32 v208, v78, v182, v208 op_sel:[0,0,0] op_sel_hi:[0,1,0]\n" \
  "v_fma_mix_f32 v209, v94, v182, v209 op_sel:[0,1,0] op_sel_hi:[0,1,0]\n" \
  "v_fma_mix_f32 v216, v15, v183, v216 op_sel:[0,0,0] op_sel_hi:[0,1,0]\n" \
  "v_fma_mix_f32 v217, v31, v183, v217 op_sel:[0,1,0] op_sel_hi:[0,1,0]\n" \
  "v_fma_mix_f32 v208, v79, v183, v208 op_sel:[0,0,0] op_sel_hi:[0,1,0]\n" \
  "v_fma_mix_f32 v209, v95, v183, v209 op_sel:[0,1,0] op_sel_hi:[0,1,0]\n" \
  "v_mfma_f32_32x32x4_2b_f16 v[0:31], v[228:229], v[228:229], 0\n" \
  "v_mfma_f32_32x32x4_2b_f16 v[64:95], v[228:229], v[228:229], 0\n" \
  "s_waitcnt lgkmcnt(3)\n" \
  "v_fma_mix_f32 v210, v96, v184, 0 op_sel:[0,0,0] op_sel_hi:[0,1,0]\n" \
  "v_fma_mix_f32 v211, v112, v184, 0 op_sel:[0,1,0] op_sel_hi:[0,1,0]\n" \
  "v_fma_mix_f32 v210, v97, v185, v210 op_sel:[0,0,0] op_sel_hi:[0,1,0]\n" \
  "v_fma_mix_f32 v211, v113, v185, v211 op_sel:[0,1,0] op_sel_hi:[0,1,0]\n" \
  "v_fma_mix_f32 v210, v98, v186, v210 op_sel:[0,0,0] op_sel_hi:[0,1,0]\n" \
  "v_fma_mix_f32 v211, v114, v186, v211 op_sel:[0,1,0] op_sel_hi:[0,1,0]\n" \
  "v_fma_mix_f32 v210, v99, v187, v210 op_sel:[0,0,0] op_sel_hi:[0,1,0]\n" \
  "v_fma_mix_f32 v211, v115, v187, v211 op_sel:[0,1,0] op_sel_hi:[0,1,0]\n" \
  "s_waitcnt lgkmcnt(2)\n" \
  "v_fma_mix_f32 v210, v100, v188, v210 op_sel:[0,0,0] op_sel_hi:[0,1,0]\n" \
  "v_fma_mix_f32 v211, v116, v188, v211 op_sel:[0,1,0] op_sel_hi:[0,1,0]\n" \
  "v_fma_mix_f32 v210, v101, v189, v210 op_sel:[0,0,0] op_sel_hi:[0,1,0]\n" \
  "v_fma_mix_f32 v211, v117, v189, v211 op_sel:[0,1,0] op_sel_hi:[0,1,0]\n" \
  "v_fma_mix_f32 v210, v102, v190, v210 op_sel:[0,0,0] op_sel_hi:[0,1,0]\n" \
  "v_fma_mix_f32 v211, v118, v190, v211 op_sel:[0,1,0] op_sel_hi:[0,1,0]\n" \
  "v_fma_mix_f32 v210, v103, v191, v210 op_sel:[0,0,0] op_sel_hi:[0,1,0]\n" \
  "v_fma_mix_f32 v211, v119, v191, v211 op_sel:[0,1,0] op_sel_hi:[0,1,0]\n" \
  "s_waitcnt lgkmcnt(1)\n" \
  "v_fma_mix_f32 v210, v104, v200, v210 op_sel:[0,0,0] op_sel_hi:[0,1,0]\n" \
  "v_fma_mix_f32 v211, v120, v200, v211 op_sel:[0,1,0] op_sel_hi:[0,1,0]\n" \
  "v_fma_mix_f32 v210, v105, v201, v210 op_sel:[0,0,0] op_sel_hi:[0,1,0]\n" \
  "v_fma_mix_f32 v211, v121, v201, v211 op_sel:[0,1,0] op_sel_hi:[0,1,0]\n" \
  "v_fma_mix_f32 v210, v106, v202, v210 op_sel:[0,0,0] op_sel_hi:[0,1,0]\n" \
  "v_fma_mix_f32 v211, v122, v202, v211 op_sel:[0,1,0] op_sel_hi:[0,1,0]\n" \
  "v_fma_mix_f32 v210, v107, v203, v210 op_sel:[0,0,0] op_sel_hi:[0,1,0]\n" \
  "v_fma_mix_f32 v211, v123, v203, v211 op_sel:[0,1,0] op_sel_hi:[0,1,0]\n" \
  "s_waitcnt lgkmcnt(0)\n" \
  "v_fma_mix_f32 v210, v108, v204, v210 op_sel:[0,0,0] op_sel_hi:[0,1,0]\n" \
  "v_fma_mix_f32 v211, v124, v204, v211 op_sel:[0,1,0] op_sel_hi:[0,1,0]\n" \
  "v_fma_mix_f32 v210, v109, v205, v210 op_sel:[0,0,0] op_sel_hi:[0,1,0]\n" \
  "v_fma_mix_f32 v211, v125, v205, v211 op_sel:[0,1,0] op_sel_hi:[0,1,0]\n" \
  "v_fma_mix_f32 v210, v110, v206, v210 op_sel:[0,0,0] op_sel_hi:[0,1,0]\n" \
  "v_fma_mix_f32 v211, v126, v206, v211 op_sel:[0,1,0] op_sel_hi:[0,1,0]\n" \
  "v_fma_mix_f32 v210, v111, v207, v210 op_sel:[0,0,0] op_sel_hi:[0,1,0]\n" \
  "v_fma_mix_f32 v211, v127, v207, v211 op_sel:[0,1,0] op_sel_hi:[0,1,0]\n" \
  "v_mfma_f32_32x32x4_2b_f16 v[96:127], v[228:229], v[228:229], 0\n" \
  "v_cvt_pk_bf16_f32 v220, v216, v217\n" \
  "v_cvt_pk_bf16_f32 v221, v218, v219\n" \
  "v_cvt_pk_bf16_f32 v222, v208, v209\n" \
  "v_cvt_pk_bf16_f32 v223, v210, v211\n" \
  "ds_write2_b32 v240, v220, v222 offset0:64 offset1:96\n" \
  "ds_write_b32 v240, v221 offset:1280\n" \
  "ds_write_b32 v240, v223 offset:1408\n" \
  "s_sub_u32 s92, s92, 0x200\n" \
  "s_subb_u32 s93, s93, 0\n" \
  "s_bitcmp0_b32 s64, 16\n" \
  "s_cbranch_scc1 Lend_%=\n" \
  "s_setprio 1\n" \
  "s_cmp_eq_u32 %[gen], 0\n" \
  "s_waitcnt lgkmcnt(0)\n" \
  "global_load_dwordx4 v[220:223], v233, s[92:93] offset:-512\n" \
  "s_cbranch_scc1 Lret3_%=\n" \
  "s_add_u32 s94, s92, 0x2000000\n" \
  "s_addc_u32 s95, s93, 0\n" \
  "global_load_dwordx2 v[230:231], v233, s[94:95] offset:-512\n" \
  "s_branch Lret3_%=\n" \
  "Lpc3_%=:\n" \
  "s_mov_b32 m0, s81\n" \
  "s_bitcmp0_b32 s73, 4\n" \
  "s_cselect_b32 exec_hi, -1, 0\n" \
  "global_load_lds_dwordx4 v224, s[74:75]\n" \
  "s_mov_b32 exec_hi, -1\n" \
  "s_add_u32 s81, s81, 0x400\n" \
  "s_lshr_b32 s73, s73, 1\n" \
  "s_mov_b32 s88, s80\n" \
  "v_mov_b32 v224, v225\n" \
  "v_mov_b32 v225, v226\n" \
  "v_mov_b32 v226, v227\n" \
  "s_branch Lpr3_%=\n" \
  "Lwt3_%=:\n" \
  "s_bcnt1_i32_b32 s82, s91\n" \
  "s_cmp_eq_u32 s82, 3\n" \
  "s_waitcnt vmcnt(35)\n" \
  "s_cbranch_scc1 Lwr3_%=\n" \
  "s_cmp_eq_u32 s82, 2\n" \
  "s_waitcnt vmcnt(34)\n" \
  "s_cbranch_scc1 Lwr3_%=\n" \
  "s_waitcnt vmcnt(33)\n" \
  "s_branch Lwr3_%=\n" \
  ".p2align 3\n" \
  "Lfl_%=:\n" \
  "s_mov_b32 m0, s81\n" \
  "s_bitcmp0_b32 s73, 4\n" \
  "s_cselect_b32 exec_hi, -1, 0\n" \
  "global_load_lds_dwordx4 v224, s[74:75]\n" \
  "s_mov_b32 exec_hi, -1\n" \
  "s_add_u32 s81, s81, 0x400\n" \
  "s_lshr_b32 s73, s73, 1\n" \
  "s_mov_b32 s88, s80\n" \
  "v_mov_b32 v224, v225\n" \
  "v_mov_b32 v225, v226\n" \
  "v_mov_b32 v226, v227\n" \
  "Lasub_%=:\n" \
  "s_bitcmp1_b32 s73, 0\n" \
  "s_cbranch_scc1 Lfl_%=\n" \
  "s_mov_b32 s68, s69\n" \
  "v_readlane_b32 s69, %[ninf], s72\n" \
  "v_mbcnt_lo_u32_b32 v102, -1, 0\n" \
  "v_mbcnt_hi_u32_b32 v102, -1, v102\n" \
  "s_add_u32 s72, s72, 1\n" \
  "s_and_b32 s82, s68, 0xff\n" \
  "s_lshl_b32 s82, s82, 9\n" \
  "s_add_u32 s82, s82, %[l_a]\n" \
  "v_lshl_add_u32 v100, v102, 4, s82\n" \
  "s_and_b32 s83, s69, 0xff\n" \
  "s_lshl_b32 s83, s83, 6\n" \
  "s_add_u32 s83, s83, %[l_map]\n" \
  "v_lshrrev_b32 v101, 1, v102\n" \
  "v_lshl_add_u32 v101, v101, 2, s83\n" \
  "s_bfe_u32 s85, s68, 0x80008\n" \
  "ds_read2_b32 v[224:225], v101 offset1:32\n" \
  "ds_read2_b32 v[226:227], v101 offset0:64 offset1:96\n" \
  "s_bitcmp0_b32 s85, 0\n" \
  "s_cselect_b32 s86, -1, 0\n" \
  "v_and_b32 v102, 1, v102\n" \
  "v_mov_b32 v101, 0\n" \
  "s_sub_u32 s82, s80, s88\n" \
  "s_and_b32 s82, s82, 63\n" \
  "s_cmp_lt_u32 s82, 2\n" \
  "s_cbranch_scc1 Lw1_%=\n" \
  "Lw0_%=:\n" \
  "s_waitcnt vmcnt(32)\n" \
  "s_cmp_gt_u32 s85, 4\n" \
  "s_cbranch_scc1 Lc34_%=\n" \
  "s_cmp_eq_u32 s85, 0\n" \
  "s_cbranch_scc1 Ldq_%=\n" \
  "ds_read_b128 v[64:67], v100 offset:0\n" \
  "ds_read_b128 v[72:75], v100 offset:1024\n" \
  "s_waitcnt lgkmcnt(1)\n" \
  "v_lshlrev_b32 v68, 16, v64\n" \
  "v_lshlrev_b32 v69, 16, v65\n" \
  "v_lshlrev_b32 v70, 16, v66\n" \
  "v_lshlrev_b32 v71, 16, v67\n" \
  "v_max3_f32 v101, |v64|, |v65|, v101\n" \
  "v_max3_f32 v101, |v66|, |v67|, v101\n" \
  "v_max3_f32 v101, |v68|, |v69|, v101\n" \
  "v_max3_f32 v101, |v70|, |v71|, v101\n" \
  "s_waitcnt lgkmcnt(0)\n" \
  "v_lshlrev_b32 v76, 16, v72\n" \
  "v_lshlrev_b32 v77, 16, v73\n" \
  "v_lshlrev_b32 v78, 16, v74\n" \
  "v_lshlrev_b32 v79, 16, v75\n" \
  "v_max3_f32 v101, |v72|, |v73|, v101\n" \
  "v_max3_f32 v101, |v74|, |v75|, v101\n" \
  "v_max3_f32 v101, |v76|, |v77|, v101\n" \
  "v_max3_f32 v101, |v78|, |v79|, v101\n" \
  "v_and_b32 v75, 0xffff0000, v75\n" \
  "v_and_b32 v74, 0xffff0000, v74\n" \
  "v_max_u32_dpp v101, v101, v101 row_shr:1\n" \
  "v_and_b32 v73, 0xffff0000, v73\n" \
  "v_and_b32 v72, 0xffff0000, v72\n" \
  "v_max_u32_dpp v101, v101, v101 row_shr:2\n" \
  "v_and_b32 v67, 0xffff0000, v67\n" \
  "v_and_b32 v66, 0xffff0000, v66\n" \
  "v_max_u32_dpp v101, v101, v101 row_shr:4\n" \
  "v_and_b32 v65, 0xffff0000, v65\n" \
  "v_and_b32 v64, 0xffff0000, v64\n" \
  "v_max_u32_dpp v101, v101, v101 row_shr:8\n" \
  "s_nop 1\n" \
  "v_max_u32_dpp v101, v101, v101 row_bcast:15 row_mask:0xa\n" \
  "s_nop 1\n" \
  "v_max_u32_dpp v101, v101, v101 row_bcast:31 row_mask:0xc\n" \
  "s_nop 0\n" \
  "v_readlane_b32 s87, v101, 63\n" \
  "s_bfe_u32 s82, s68, 0x8000e\n" \
  "v_mov_b32 v101, s82\n" \
  "s_lshr_b32 s87, s87, 23\n" \
  "s_max_u32 s87, s87, 16\n" \
  "s_lshl_b32 s82, s87, 18\n" \
  "s_sub_u32 s87, 0x105, s87\n" \
  "s_lshl_b32 s87, s87, 23\n" \
  "v_mov_b32 v103, s82\n" \
  "s_mov_b64 exec, 1\n" \
  "ds_add_u32 v101, v103\n" \
  "s_mov_b64 exec, -1\n" \
  "v_fma_mixlo_f16 v68, v68, s87, 0\n" \
  "v_fma_mixlo_f16 v69, v69, s87, 0\n" \
  "v_fma_mixlo_f16 v70, v70, s87, 0\n" \
  "v_fma_mixlo_f16 v71, v71, s87, 0\n" \
  "v_fma_mixhi_f16 v68, v64, s87, 0\n" \
  "v_fma_mixhi_f16 v69, v65, s87, 0\n" \
  "v_fma_mixhi_f16 v70, v66, s87, 0\n" \
  "v_fma_mixhi_f16 v71, v67, s87, 0\n" \
  "ds_write_b128 v100, v[68:71] offset:0\n" \
  "v_fma_mixlo_f16 v76, v76, s87, 0\n" \
  "v_fma_mixlo_f16 v77, v77, s87, 0\n" \
  "v_fma_mixlo_f16 v78, v78, s87, 0\n" \
  "v_fma_mixlo_f16 v79, v79, s87, 0\n" \
  "v_fma_mixhi_f16 v76, v72, s87, 0\n" \
  "v_fma_mixhi_f16 v77, v73, s87, 0\n" \
  "v_fma_mixhi_f16 v78, v74, s87, 0\n" \
  "v_fma_mixhi_f16 v79, v75, s87, 0\n" \
  "ds_write_b128 v100, v[76:79] offset:1024\n" \
  "Ldq_%=:\n" \
  "s_and_b32 s82, s69, 0xff\n" \
  "s_lshl_b32 s82, s82, 9\n" \
  "s_add_u32 s81, s82, %[l_a]\n" \
  "s_lshr_b32 s73, s69, 24\n" \
  "s_waitcnt lgkmcnt(0)\n" \
  "v_readfirstlane_b32 s82, v224\n" \
  "v_cmp_eq_u32 vcc, 0, v224\n" \
  "v_cmp_eq_u32 s[86:87], 0, v225\n" \
  "v_mov_b32 v100, s82\n" \
  "v_lshlrev_b32 v102, 4, v102\n" \
  "v_cndmask_b32 v224, v224, v100, vcc\n" \
  "v_cndmask_b32 v225, v225, v100, s[86:87]\n" \
  "v_cmp_eq_u32 vcc, 0, v226\n" \
  "v_cmp_eq_u32 s[86:87], 0, v227\n" \
  "v_or_b32 v224, v224, v102\n" \
  "v_or_b32 v225, v225, v102\n" \
  "v_cndmask_b32 v226, v226, v100, vcc\n" \
  "v_cndmask_b32 v227, v227, v100, s[86:87]\n" \
  "v_or_b32 v226, v226, v102\n" \
  "v_or_b32 v227, v227, v102\n" \
  "Ldx_%=:\n" \
  "s_setpc_b64 s[70:71]\n" \
  "Lw1_%=:\n" \
  "s_cmp_eq_u32 s82, 1\n" \
  "s_waitcnt vmcnt(16)\n" \
  "s_cbranch_scc1 Lw0_%=\n" \
  "s_waitcnt vmcnt(0)\n" \
  "s_branch Lw0_%=\n" \
  "Lc34_%=:\n" \
  "s_cmp_gt_u32 s85, 6\n" \
  "s_cbranch_scc1 Lc4_%=\n" \
  "ds_read_b128 v[64:67], v100 offset:0\n" \
  "ds_read_b128 v[72:75], v100 offset:1024\n" \
  "ds_read_b128 v[80:83], v100 offset:2048\n" \
  "s_waitcnt lgkmcnt(2)\n" \
  "v_lshlrev_b32 v68, 16, v64\n" \
  "v_lshlrev_b32 v69, 16, v65\n" \
  "v_lshlrev_b32 v70, 16, v66\n" \
  "v_lshlrev_b32 v71, 16, v67\n" \
  "v_max3_f32 v101, |v64|, |v65|, v101\n" \
  "v_max3_f32 v101, |v66|, |v67|, v101\n" \
  "v_max3_f32 v101, |v68|, |v69|, v101\n" \
  "v_max3_f32 v101, |v70|, |v71|, v101\n" \
  "s_waitcnt lgkmcnt(1)\n" \
  "v_lshlrev_b32 v76, 16, v72\n" \
  "v_lshlrev_b32 v77, 16, v73\n" \
  "v_lshlrev_b32 v78, 16, v74\n" \
  "v_lshlrev_b32 v79, 16, v75\n" \
  "v_max3_f32 v101, |v72|, |v73|, v101\n" \
  "v_max3_f32 v101, |v74|, |v75|, v101\n" \
  "v_max3_f32 v101, |v76|, |v77|, v101\n" \
  "v_max3_f32 v101, |v78|, |v79|, v101\n" \
  "s_waitcnt lgkmcnt(0)\n" \
  "v_lshlrev_b32 v84, 16, v80\n" \
  "v_lshlrev_b32 v85, 16, v81\n" \
  "v_lshlrev_b32 v86, 16, v82\n" \
  "v_lshlrev_b32 v87, 16, v83\n" \
  "s_mov_b32 exec_hi, s86\n" \
  "v_max3_f32 v101, |v80|, |v81|, v101\n" \
  "v_max3_f32 v101, |v82|, |v83|, v101\n" \
  "v_max3_f32 v101, |v84|, |v85|, v101\n" \
  "v_max3_f32 v101, |v86|, |v87|, v101\n" \
  "s_mov_b32 exec_hi, -1\n" \
  "v_and_b32 v83, 0xffff0000, v83\n" \
  "v_and_b32 v82, 0xffff0000, v82\n" \
  "v_max_u32_dpp v101, v101, v101 row_shr:1\n" \
  "v_and_b32 v81, 0xffff0000, v81\n" \
  "v_and_b32 v80, 0xffff0000, v80\n" \
  "v_max_u32_dpp v101, v101, v101 row_shr:2\n" \
  "v_and_b32 v75, 0xffff0000, v75\n" \
  "v_and_b32 v74, 0xffff0000, v74\n" \
  "v_max_u32_dpp v101, v101, v101 row_shr:4\n" \
  "v_and_b32 v73, 0xffff0000, v73\n" \
  "v_and_b32 v72, 0xffff0000, v72\n" \
  "v_max_u32_dpp v101, v101, v101 row_shr:8\n" \
  "v_and_b32 v67, 0xffff0000, v67\n" \
  "v_and_b32 v66, 0xffff0000, v66\n" \
  "v_max_u32_dpp v101, v101, v101 row_bcast:15 row_mask:0xa\n" \
  "v_and_b32 v65, 0xffff0000, v65\n" \
  "v_and_b32 v64, 0xffff0000, v64\n" \
  "v_max_u32_dpp v101, v101, v101 row_bcast:31 row_mask:0xc\n" \
  "s_nop 0\n" \
  "v_readlane_b32 s87, v101, 63\n" \
  "s_bfe_u32 s82, s68, 0x8000e\n" \
  "v_mov_b32 v101, s82\n" \
  "s_lshr_b32 s87, s87, 23\n" \
  "s_max_u32 s87, s87, 16\n" \
  "s_lshl_b32 s82, s87, 18\n" \
  "s_sub_u32 s87, 0x105, s87\n" \
  "s_lshl_b32 s87, s87, 23\n" \
  "v_mov_b32 v103, s82\n" \
  "s_mov_b64 exec, 1\n" \
  "ds_add_u32 v101, v103\n" \
  "s_mov_b64 exec, -1\n" \
  "v_fma_mixlo_f16 v68, v68, s87, 0\n" \
  "v_fma_mixlo_f16 v69, v69, s87, 0\n" \
  "v_fma_mixlo_f16 v70, v70, s87, 0\n" \
  "v_fma_mixlo_f16 v71, v71, s87, 0\n" \
  "v_fma_mixhi_f16 v68, v64, s87, 0\n" \
  "v_fma_mixhi_f16 v69, v65, s87, 0\n" \
  "v_fma_mixhi_f16 v70, v66, s87, 0\n" \
  "v_fma_mixhi_f16 v71, v67, s87, 0\n" \
  "ds_write_b128 v100, v[68:71] offset:0\n" \
  "v_fma_mixlo_f16 v76, v76, s87, 0\n" \
  "v_fma_mixlo_f16 v77, v77, s87, 0\n" \
  "v_fma_mixlo_f16 v78, v78, s87, 0\n" \
  "v_fma_mixlo_f16 v79, v79, s87, 0\n" \
  "v_fma_mixhi_f16 v76, v72, s87, 0\n" \
  "v_fma_mixhi_f16 v77, v73, s87, 0\n" \
  "v_fma_mixhi_f16 v78, v74, s87, 0\n" \
  "v_fma_mixhi_f16 v79, v75, s87, 0\n" \
  "ds_write_b128 v100, v[76:79] offset:1024\n" \
  "v_fma_mixlo_f16 v84, v84, s87, 0\n" \
  "v_fma_mixlo_f16 v85, v85, s87, 0\n" \
  "v_fma_mixlo_f16 v86, v86, s87, 0\n" \
  "v_fma_mixlo_f16 v87, v87, s87, 0\n" \
  "v_fma_mixhi_f16 v84, v80, s87, 0\n" \
  "v_fma_mixhi_f16 v85, v81, s87, 0\n" \
  "v_fma_mixhi_f16 v86, v82, s87, 0\n" \
  "v_fma_mixhi_f16 v87, v83, s87, 0\n" \
  "s_mov_b32 exec_hi, s86\n" \
  "ds_write_b128 v100, v[84:87] offset:2048\n" \
  "s_mov_b32 exec_hi, -1\n" \
  "s_branch Ldq_%=\n" \
  "Lc4_%=:\n" \
  "ds_read_b128 v[64:67], v100 offset:0\n" \
  "ds_read_b128 v[72:75], v100 offset:1024\n" \
  "ds_read_b128 v[80:83], v100 offset:2048\n" \
  "ds_read_b128 v[88:91], v100 offset:3072\n" \
  "s_waitcnt lgkmcnt(3)\n" \
  "v_lshlrev_b32 v68, 16, v64\n" \
  "v_lshlrev_b32 v69, 16, v65\n" \
  "v_lshlrev_b32 v70, 16, v66\n" \
  "v_lshlrev_b32 v71, 16, v67\n" \
  "v_max3_f32 v101, |v64|, |v65|, v101\n" \
  "v_max3_f32 v101, |v66|, |v67|, v101\n" \
  "v_max3_f32 v101, |v68|, |v69|, v101\n" \
  "v_max3_f32 v101, |v70|, |v71|, v101\n" \
  "s_waitcnt lgkmcnt(2)\n" \
  "v_lshlrev_b32 v76, 16, v72\n" \
  "v_lshlrev_b32 v77, 16, v73\n" \
  "v_lshlrev_b32 v78, 16, v74\n" \
  "v_lshlrev_b32 v79, 16, v75\n" \
  "v_max3_f32 v101, |v72|, |v73|, v101\n" \
  "v_max3_f32 v101, |v74|, |v75|, v101\n" \
  "v_max3_f32 v101, |v76|, |v77|, v101\n" \
  "v_max3_f32 v101, |v78|, |v79|, v101\n" \
  "s_waitcnt lgkmcnt(1)\n" \
  "v_lshlrev_b32 v84, 16, v80\n" \
  "v_lshlrev_b32 v85, 16, v81\n" \
  "v_lshlrev_b32 v86, 16, v82\n" \
  "v_lshlrev_b32 v87, 16, v83\n" \
  "v_max3_f32 v101, |v80|, |v81|, v101\n" \
  "v_max3_f32 v101, |v82|, |v83|, v101\n" \
  "v_max3_f32 v101, |v84|, |v85|, v101\n" \
  "v_max3_f32 v101, |v86|, |v87|, v101\n" \
  "s_waitcnt lgkmcnt(0)\n" \
  "v_lshlrev_b32 v92, 16, v88\n" \
  "v_lshlrev_b32 v93, 16, v89\n" \
  "v_lshlrev_b32 v94, 16, v90\n" \
  "v_lshlrev_b32 v95, 16, v91\n" \
  "s_mov_b32 exec_hi, s86\n" \
  "v_max3_f32 v101, |v88|, |v89|, v101\n" \
  "v_max3_f32 v101, |v90|, |v91|, v101\n" \
  "v_max3_f32 v101, |v92|, |v93|, v101\n" \
  "v_max3_f32 v101, |v94|, |v95|, v101\n" \
  "s_mov_b32 exec_hi, -1\n" \
  "v_and_b32 v91, 0xffff0000, v91\n" \
  "v_and_b32 v90, 0xffff0000, v90\n" \
  "v_max_u32_dpp v101, v101, v101 row_shr:1\n" \
  "v_and_b32 v89, 0xffff0000, v89\n" \
  "v_and_b32 v88, 0xffff0000, v88\n" \
  "v_max_u32_dpp v101, v101, v101 row_shr:2\n" \
  "v_and_b32 v83, 0xffff0000, v83\n" \
  "v_and_b32 v82, 0xffff0000, v82\n" \
  "v_max_u32_dpp v101, v101, v101 row_shr:4\n" \
  "v_and_b32 v81, 0xffff0000, v81\n" \
  "v_and_b32 v80, 0xffff0000, v80\n" \
  "v_max_u32_dpp v101, v101, v101 row_shr:8\n" \
  "v_and_b32 v75, 0xffff0000, v75\n" \
  "v_and_b32 v74, 0xffff0000, v74\n" \
  "v_max_u32_dpp v101, v101, v101 row_bcast:15 row_mask:0xa\n" \
  "v_and_b32 v73, 0xffff0000, v73\n" \
  "v_and_b32 v72, 0xffff0000, v72\n" \
  "v_max_u32_dpp v101, v101, v101 row_bcast:31 row_mask:0xc\n" \
  "v_and_b32 v64, 0xffff0000, v64\n" \
  "v_and_b32 v65, 0xffff0000, v65\n" \
  "v_and_b32 v66, 0xffff0000, v66\n" \
  "v_and_b32 v67, 0xffff0000, v67\n" \
  "s_nop 0\n" \
  "v_readlane_b32 s87, v101, 63\n" \
  "s_bfe_u32 s82, s68, 0x8000e\n" \
  "v_mov_b32 v101, s82\n" \
  "s_lshr_b32 s87, s87, 23\n" \
  "s_max_u32 s87, s87, 16\n" \
  "s_lshl_b32 s82, s87, 18\n" \
  "s_sub_u32 s87, 0x105, s87\n" \
  "s_lshl_b32 s87, s87, 23\n" \
  "v_mov_b32 v103, s82\n" \
  "s_mov_b64 exec, 1\n" \
  "ds_add_u32 v101, v103\n" \
  "s_mov_b64 exec, -1\n" \
  "v_fma_mixlo_f16 v68, v68, s87, 0\n" \
  "v_fma_mixlo_f16 v69, v69, s87, 0\n" \
  "v_fma_mixlo_f16 v70, v70, s87, 0\n" \
  "v_fma_mixlo_f16 v71, v71, s87, 0\n" \
  "v_fma_mixhi_f16 v68, v64, s87, 0\n" \
  "v_fma_mixhi_f16 v69, v65, s87, 0\n" \
  "v_fma_mixhi_f16 v70, v66, s87, 0\n" \
  "v_fma_mixhi_f16 v71, v67, s87, 0\n" \
  "ds_write_b128 v100, v[68:71] offset:0\n" \
  "v_fma_mixlo_f16 v76, v76, s87, 0\n" \
  "v_fma_mixlo_f16 v77, v77, s87, 0\n" \
  "v_fma_mixlo_f16 v78, v78, s87, 0\n" \
  "v_fma_mixlo_f16 v79, v79, s87, 0\n" \
  "v_fma_mixhi_f16 v76, v72, s87, 0\n" \
  "v_fma_mixhi_f16 v77, v73, s87, 0\n" \
  "v_fma_mixhi_f16 v78, v74, s87, 0\n" \
  "v_fma_mixhi_f16 v79, v75, s87, 0\n" \
  "ds_write_b128 v100, v[76:79] offset:1024\n" \
  "v_fma_mixlo_f16 v84, v84, s87, 0\n" \
  "v_fma_mixlo_f16 v85, v85, s87, 0\n" \
  "v_fma_mixlo_f16 v86, v86, s87, 0\n" \
  "v_fma_mixlo_f16 v87, v87, s87, 0\n" \
  "v_fma_mixhi_f16 v84, v80, s87, 0\n" \
  "v_fma_mixhi_f16 v85, v81, s87, 0\n" \
  "v_fma_mixhi_f16 v86, v82, s87, 0\n" \
  "v_fma_mixhi_f16 v87, v83, s87, 0\n" \
  "ds_write_b128 v100, v[84:87] offset:2048\n" \
  "v_fma_mixlo_f16 v92, v92, s87, 0\n" \
  "v_fma_mixlo_f16 v93, v93, s87, 0\n" \
  "v_fma_mixlo_f16 v94, v94, s87, 0\n" \
  "v_fma_mixlo_f16 v95, v95, s87, 0\n" \
  "v_fma_mixhi_f16 v92, v88, s87, 0\n" \
  "v_fma_mixhi_f16 v93, v89, s87, 0\n" \
  "v_fma_mixhi_f16 v94, v90, s87, 0\n" \
  "v_fma_mixhi_f16 v95, v91, s87, 0\n" \
  "s_mov_b32 exec_hi, s86\n" \
  "ds_write_b128 v100, v[92:95] offset:3072\n" \
  "s_mov_b32 exec_hi, -1\n" \
  "s_branch Ldq_%=\n" \
  "Lend_%=:\n" \
  "s_mov_b32 m0, s90\n" \
  "s_setprio 0\n" \
  "s_waitcnt vmcnt(0) lgkmcnt(0)\n" \
  "s_nop 7\n" \
  "s_nop 7\n" \
  "s_nop 7\n" \
  ""
#define MAIN_CLOB "v0", "v1", "v2", "v3", "v4", "v5", "v6", "v7", "v8", "v9", "v10", "v11", "v12", "v13", "v14", "v15", "v16", "v17", "v18", "v19", "v20", "v21", "v22", "v23", "v24", "v25", "v26", "v27", "v28", "v29", "v30", "v31", "v32", "v33", "v34", "v35", "v36", "v37", "v38", "v39", "v40", "v41", "v42", "v43", "v44", "v45", "v46", "v47", "v48", "v49", "v50", "v51", "v52", "v53", "v54", "v55", "v56", "v57", "v58", "v59", "v60", "v61", "v62", "v63", "v64", "v65", "v66", "v67", "v68", "v69", "v70", "v71", "v72", "v73", "v74", "v75", "v76", "v77", "v78", "v79", "v80", "v81", "v82", "v83", "v84", "v85", "v86", "v87", "v88", "v89", "v90", "v91", "v92", "v93", "v94", "v95", "v96", "v97", "v98", "v99", "v100", "v101", "v102", "v103", "v104", "v105", "v106", "v107", "v108", "v109", "v110", "v111", "v112", "v113", "v114", "v115", "v116", "v117", "v118", "v119", "v120", "v121", "v122", "v123", "v124", "v125", "v126", "v127", "v128", "v129", "v130", "v131", "v132", "v133", "v134", "v135", "v136", "v137", "v138", "v139", "v140", "v141", "v142", "v143", "v144", "v145", "v146", "v147", "v148", "v149", "v150", "v151", "v152", "v153", "v154", "v155", "v156", "v157", "v158", "v159", "v160", "v161", "v162", "v163", "v164", "v165", "v166", "v167", "v168", "v169", "v170", "v171", "v172", "v173", "v174", "v175", "v176", "v177", "v178", "v179", "v180", "v181", "v182", "v183", "v184", "v185", "v186", "v187", "v188", "v189", "v190", "v191", "v192", "v193", "v194", "v195", "v196", "v197", "v198", "v199", "v200", "v201", "v202", "v203", "v204", "v205", "v206", "v207", "v208", "v209", "v210", "v211", "v212", "v213", "v214", "v215", "v216", "v217", "v218", "v219", "v220", "v221", "v222", "v223", "v224", "v225", "v226", "v227", "v228", "v229", "v230", "v231", "v232", "v233", "v234", "v235", "v236", "v237", "v238", "v239", "v240", "v241", "s64", "s65", "s66", "s67", "s68", "s69", "s70", "s71", "s72", "s73", "s74", "s75", "s76", "s77", "s78", "s79", "s80", "s81", "s82", "s83", "s84", "s85", "s86", "s87", "s88", "s89", "s90", "s91", "s92", "s93", "s94", "s95", "s96", "s97", "vcc", "scc", "memory"
__global__ __launch_bounds__(512, 2) void k_main(const u16* __restrict__ x, const u16* __restrict__ wrad, const u32x4* __restrict__ recA,
                                                 const u32* __restrict__ recM, const u16* __restrict__ tab, const u32x4* __restrict__ wfrag,
                                                 const float* __restrict__ epsf, u16* __restrict__ out, const u16* __restrict__ xh,
                                                 const u32x4* __restrict__ exb, const u32x4* __restrict__ wimg, const u32x4* __restrict__ ea, u32x4* spill, int N, u32 M, int EL) {
  __shared__ __attribute__((aligned(16))) char smem[L_TOTAL];
  const u32 LB = (u32)(uintptr_t)(__attribute__((address_space(3))) char*)smem;
  const int t = threadIdx.x, lane = t & 63;
  const int w = __builtin_amdgcn_readfirstlane(t >> 6);
  const int b = (blockIdx.x & 7) * 32 + (blockIdx.x >> 3);
  TS(1, 0);
  const int st = tab[t * TABS + b], cn = tab[t * TABS + b + 1] - st;
  const int t2 = 512 + (t & 127);
  const u32x4 ex0 = exb[t], ex1 = exb[t2], wi0 = wimg[t], wi1 = wimg[t2];
#define PFS 192
#define RA 6
#define SPB blockIdx.x
#define SPS 544
#define TKT 1
#define PFA 1
  u32 pfc;
  {
    const char* pc; int len;
    asm volatile("s_getpc_b64 %0\n.Lkm_pc:\n\ts_mov_b32 %1, .Lkm_end-.Lkm_pc" : "=s"(pc), "=s"(len));
    pfc = *(const __attribute__((address_space(1))) u32*)(pc + max(min(t * 128, len), 0));
  }
  const u32x2 eg = *(const u32x2*)(epsf + 28);
  const int ew = eg[0], gen = eg[1];
  const int n0 = (int)min((u32)N, (((u32)b << 24) + M - 1) / M);
  const int n1 = b == NBK - 1 ? N : (int)min((u32)N, (((u32)(b + 1) << 24) + M - 1) / M);
  const int nn = n1 - n0;
  {
    const int nclr = (L_TOTAL - L_CNT) / 16;
#pragma unroll
    for (int i = 0; i < (nclr + 511) / 512; ++i)
      if (i * 512 + t < nclr) *LDSP(u32x4, LB + L_CNT + (i * 512 + t) * 16) = u32x4{0, 0, 0, 0};
  }
  const int inc = wave_incl_scan(cn);
  const int tw = __builtin_amdgcn_readlane(inc, 63);
  const bool okw = tw <= NSL * 64;
  const u32 wm = LB + L_MAP + w * (NSL * 256);
  const int r0 = t * CHCAP + st;
  if (okw) for (int i = 0; i < cn; ++i) *LDSP(int, wm + (inc - cn + i) * 4) = r0 + i;
  TS(1, 1);
  u32x3 mv[NSL]; int ri[NSL], rk[NSL], es[NSL];
  const int twv = okw ? tw : 0;
#pragma unroll
  for (int k = 0; k < NSL; ++k) {
    ri[k] = *LDSP(const int, wm + (k * 64 + lane) * 4);
    if (k * 64 + lane >= twv) ri[k] = 0;
  }
  asm volatile("" ::"v"(ex0), "v"(ex1), "v"(wi0), "v"(wi1), "v"(pfc));
#pragma unroll
  for (int k = 0; k < NSL; ++k) asm volatile("global_load_dwordx3 %0, %1, %2" : "=v"(mv[k]) : "v"(ri[k] * 12), "s"(recM));
  u32 pfa = 0;
  *LDSP(u32x4, wm + lane * 16) = u32x4{0, 0, 0, 0};
  if (lane < NSL * 16 - 64) *LDSP(u32x4, wm + 1024 + lane * 16) = u32x4{0, 0, 0, 0};
  *LDSP(u32x4, LB + L_W + t * 16) = ex0;
  if (t < 128) *LDSP(u32x4, LB + L_W + t2 * 16) = ex1;
  bar();
  TS(1, 2);
  if (PFS) asm volatile("global_load_dword %0, %1, %2" : "+{v253}"(pfa) : "v"(max(EL - (int)blockIdx.x * PFS - min(t, PFS - 1), 0) * 128), "s"(ea));
  if (!okw) *LDSP(int, LB + L_KM + 256) = 1;
  const u32 idle = (48 + (lane & 15)) * 4;
  u32 l4[NSL];
  auto rank = [&](const int k) AINL {
    l4[k] = k * 64 + lane < twv ? (((mv[k][1] >> 16) - n0) & 63) * 4 : idle;
    rk[k] = __hip_atomic_fetch_add(LDSP(int, LB + L_CNT + l4[k] * 8 + w * 4), 1, __ATOMIC_RELAXED, __HIP_MEMORY_SCOPE_WORKGROUP);
    es[k] = *LDSP(const unsigned char, LB + L_W + (mv[k][2] & 0xffff));
  };
  auto emax = [&](const int k) AINL {
    u32 my = pkmax(mv[k][0] & 0x7fff7fffu, (mv[k][1] & 0x7fffu) | 0x3f800000u);
    int ey = max(my >> 16, my & 0xffffu) >> 7;
    __hip_atomic_fetch_max(LDSP(int, LB + L_KM + l4[k]), es[k] + ey, __ATOMIC_RELAXED, __HIP_MEMORY_SCOPE_WORKGROUP);
  };
  asm volatile("s_waitcnt vmcnt(%7)" : "+v"(mv[0]), "+v"(mv[1]), "+v"(mv[2]), "+v"(mv[3]), "+v"(mv[4]), "+v"(mv[5]), "+v"(mv[6]) : "n"(PFS ? 1 : 0));
  l4[5] = l4[6] = idle; rk[5] = rk[6] = es[5] = es[6] = 0;
  rank(0); rank(1); rank(2); rank(3); rank(4);
  if (twv > 320) { rank(5); rank(6); emax(5); emax(6); }
  emax(0); emax(1); emax(2); emax(3); emax(4);
  bar();
  TS(1, 3);
  int npre, nidx, nblk, btot, npm;
  const int kmx = *LDSP(const int, LB + L_KM + lane * 4);
  bool fast = __builtin_amdgcn_readfirstlane(*LDSP(const int, LB + L_KM + 256)) == 0 && LB == 0;
  {
    u32x4 c0 = *LDSP(const u32x4, LB + L_CNT + lane * 32), c1 = *LDSP(const u32x4, LB + L_CNT + lane * 32 + 16);
    int cc[8] = {(int)c0[0], (int)c0[1], (int)c0[2], (int)c0[3], (int)c1[0], (int)c1[1], (int)c1[2], (int)c1[3]};
    int ncnt = 0; npre = 0;
#pragma unroll
    for (int k = 0; k < 8; ++k) { if (k == w) npre = ncnt; ncnt += cc[k]; }
    nblk = max(3, (ncnt + 15) >> 4);
    npm = ncnt ? ((1 << ((nblk + 1) >> 1)) - 1) | ((nblk & 1) << (4 + (nblk >> 1))) : 0;
    int ng = lane < nn ? max(MINBLK, nblk) : 0;
    int gi = wave_incl_scan(ng);
    btot = __builtin_amdgcn_readlane(gi, 63);
    fast = fast && nn <= 44 && btot <= BCAP && __builtin_amdgcn_ballot_w64(ng > 8) == 0;
    nidx = lane < nn ? gi - ng : 0;
  }
  *LDSP(u32x4, LB + L_W + t * 16) = wi0;
  if (t < 128) *LDSP(u32x4, LB + L_W + t2 * 16) = wi1;
  const int r = lane & 31, h = lane >> 5;
  int ninf = 0;
  const u32 lb = L_LIST + w * 512;
  if (fast) {
    const int avg = (int)((float)btot * __builtin_amdgcn_rcpf((float)max(nn, 1)));
    const int key = lane < nn ? min(max(nblk - avg + 1, 0), 3) : -1;
    int pos = 0, base = 0;
#pragma unroll
    for (int k = 3; k >= 0; --k) {
      unsigned long long m = __builtin_amdgcn_ballot_w64(key == k);
      if (key == k) pos = base + __builtin_amdgcn_mbcnt_hi((u32)(m >> 32), __builtin_amdgcn_mbcnt_lo((u32)m, 0));
      base += __builtin_popcountll(m);
    }
    const int rnd = pos >> 2, ra = (((nn + 3) >> 2) * RA + 9) / 10;

    const int rb = ((nn + 3) >> 2) - ra, grp = rnd < rb, nj = grp ? rnd : rnd - rb;
    const int wv = ((rnd & 1) ? 3 - (pos & 3) : pos & 3) + 4 * grp;
    const int npk = (lane < nn ? (nidx * 16 + npre) | (nj == 0 ? 0x8000 : 0) : (BCAP + 1) * 16) | (kmx << 16);
    int pk[NSL];
#pragma unroll
    for (int k = 0; k < NSL; ++k) pk[k] = __builtin_amdgcn_ds_bpermute(l4[k], npk);
    ninf = __builtin_amdgcn_ds_permute(lane < nn && wv == w ? nj * 4 : 252, nidx | (nblk << 8) | (lane << 16) | (npm << 24));
    u32 eo[NSL];
    auto pre = [&](const int k) AINL {
      eo[k] = __builtin_amdgcn_perm(mv[k][1], mv[k][2], 0x0c070302u) * 32;
      u32 pa = (pk[k] & 0x8000) ? eo[k] : 0;
      if (PFA == 1) asm volatile("global_load_dword %0, %1, %2" : "+{v253}"(pfa) : "v"(pa), "s"(ea));
      if (PFA == 2) asm volatile("global_load_dword %0, %1, %2 nt" : "+{v253}"(pfa) : "v"(pa), "s"(ea));
    };
    auto place = [&](const int k) AINL {
      u32 slot = (pk[k] & 0x7fff) + rk[k], sj = slot & 15;
      u32 ga = LB + L_YS + (slot >> 4) * 160;
      float wy = __builtin_bit_cast(float, max(es[k] - (pk[k] >> 16) + 260, 0) << 23);
      u32 ya = ga + (sj >> 3) * 64 + (sj & 7) * 2;
      *LDSP(u32, LB + L_MAP + slot * 4) = eo[k] + 32;
      *LDSP(u16, ga + 128 + (sj & 4) * 4 + (sj & 8) + (sj & 3) * 2) = (u16)mv[k][2];
      *LDSP(_Float16, ya) = (_Float16)wy;
      *LDSP(_Float16, ya + 16) = (_Float16)(bf2f(mv[k][0]) * wy);
      *LDSP(_Float16, ya + 32) = (_Float16)(__uint_as_float(mv[k][0] & 0xffff0000u) * wy);
      *LDSP(_Float16, ya + 48) = (_Float16)(bf2f(mv[k][1]) * wy);
    };
    pre(0); pre(1); pre(2); pre(3); pre(4);
    if (lane >= 8) ninf = 0;
    int sc = (ninf >> 8) & 255;
    sc = dppadd<0x114, 0xf>(dppadd<0x112, 0xf>(dppadd<0x111, 0xf>(sc)));
    const int j4 = (lane >> 3) * 4, q = lane & 7;
    const int ci = __builtin_amdgcn_ds_bpermute(j4, ninf), pi = __builtin_amdgcn_ds_bpermute((j4 + 4) & 28, ninf), s1 = __builtin_amdgcn_ds_bpermute(j4, sc);
    const int tot2 = 2 * __builtin_amdgcn_readlane(sc, 7);
    if (twv > 320) { pre(5); pre(6); place(5); place(6); }
    place(0); place(1); place(2); place(3); place(4);
    const int nb = (ci >> 8) & 255, ep = q ? 0 : pi ? (6 << 16) | ((pi << 3) & (7 << 19)) | (pi << 24) : 2 << 16;
    const int bl = ((ci & 255) + q) * 8;
    if (q < nb) *LDSP(int, lb + (s1 - nb + q) * 4) = bl | (q == 0 && lane ? 2 << 16 : 0);
    if (q < (lane >= 56 ? MAIN_D : nb)) *LDSP(int, lb + (tot2 + q - (lane >= 56 ? 0 : s1)) * 4) = (lane >= 56 ? (pi & 255) * 8 : bl | (1 << 16)) | ep;
  }
  if (w == 0) {
    *LDSP(int, LB + L_NI + lane * 4) = fast ? (L_A + nidx * 512) | ((kmx + ew - 549) << 18) : L_W + lane * 2432;
    *LDSP(int, LB + L_NI + 256 + lane * 4) = fast ? L_YS + nidx * 160 : L_W + lane * 2432 + 2048;
  }
  bar();

  TS(1, 4);
  if (fast) {
    const int i = lane & 15;
    const u32 c_a = L_A + (h * 8 + (i >> 2)) * 32 + (((i >> 1) & 1) + 2 * ((lane >> 4) & 1)) * 8, c_y = L_YS + h * 64 + ((lane >> 2) & 1) * 16,
              c_s = L_YS + 128 + 16 * h, c_x = r * 4, c_u = L_A + h * 512;
    const int idxe = 0x88888888, idxo = 0xdddddddd;
    if (__builtin_amdgcn_readfirstlane(ninf)) asm volatile(MAIN_ASM
                 :
                 : [c_a] "v"(c_a), [c_y] "v"(c_y), [c_s] "v"(c_s), [c_x] "v"(c_x), [c_u] "v"(c_u), [ie] "v"(idxe), [io] "v"(idxo), [xp] "s"(xh), [xq] "s"(xh + (size_t)N * 128), [sp] "s"(spill + (SPB * 8 + w) * SPS),
                   [lb] "s"(lb), [gen] "s"(gen), [ra] "s"(ea), [ninf] "v"(ninf), "{v253}"(pfa), [l_a] "n"(L_A), [l_map] "n"(L_MAP), [l_ys] "n"(L_YS)
                 : MAIN_CLOB);
    else asm volatile("s_waitcnt vmcnt(0)" ::"{v253}"(pfa));
  } else {
    asm volatile("s_waitcnt vmcnt(0)" ::"{v253}"(pfa));
    for (int ln = w; ln < nn; ln += 8) {
      float U[16], tr[3] = {0.f, 0.f, 0.f};
#pragma unroll
      for (int i = 0; i < 16; ++i) U[i] = 0.f;
      for (int c = 0; c < NCH; ++c) {
        int s0 = tab[c * TABS + b], s1 = tab[c * TABS + b + 1];
        for (int q = s0; q < s1; ++q) {
          size_t ri = (size_t)c * CHCAP + q;
          u32 m0 = recM[ri * 3], m1 = recM[ri * 3 + 1], m2 = recM[ri * 3 + 2];
          if ((((m1 >> 16) - n0) & 255) != ln) continue;
          const u16* ap = (const u16*)(ea + (size_t)((m1 >> 24 << 16) | (m2 >> 16)) * 2);
          float R[5] = {0.f, 0.f, 0.f, 0.f, 0.f};
          for (int dd = 0; dd < 16; ++dd) {
            float a = bf2f(ap[dd]);
#pragma unroll
            for (int p = 0; p < 5; ++p) R[p] = fmaf(a, bf2f(wrad[dd * 320 + p * 64 + lane]), R[p]);
          }
          float Y[4] = {1.f, bf2f(m0 & 0xffff), bf2f(m0 >> 16), bf2f(m1 & 0xffff)};
#pragma unroll
          for (int tt = 0; tt < 4; ++tt) {
            float f = bf2f(x[(size_t)(m2 & 0xffff) * 256 + tt * 64 + lane]);
#pragma unroll
            for (int s = 0; s < 4; ++s) U[s * 4 + tt] += Y[s] * f * R[tt == 0 ? (s == 0 ? 0 : 2) : (s == 0 ? 3 : s == tt ? 1 : 4)];
            if (tt > 0) tr[tt - 1] += Y[tt] * f * R[4];
          }
        }
      }
      u32 ua = LB + L_W + ln * 2432 + ((lane * 2) ^ ((ln & 7) * 16));
#pragma unroll
      for (int i = 0; i < 16; ++i) *LDSP(u16, ua + i * 128) = (u16)cvtpk(U[i], 0.f);
      *LDSP(u16, ua + 16 * 128) = (u16)cvtpk(tr[1], 0.f);
      *LDSP(u16, ua + 17 * 128) = (u16)cvtpk(tr[0], 0.f);
      *LDSP(u16, ua + 18 * 128) = (u16)cvtpk(tr[2], 0.f);
    }
  }
  TS(1, 5);
  TC(1, 8);
  int unit = w;
  if (TKT && fast) {
    if (lane == 0) unit = __hip_atomic_fetch_add(LDSP(int, LB + L_KM + 260), 1, __ATOMIC_RELAXED, __HIP_MEMORY_SCOPE_WORKGROUP);
    unit = __builtin_amdgcn_readfirstlane(unit);
  }
  const int tile = unit >> 1, uu = unit & 1;
  u32x4 wf[5][2][2];
  if (tile * 16 < nn) {
#pragma unroll
    for (int blk = 0; blk < 5; ++blk)
#pragma unroll
      for (int ks = 0; ks < 2; ++ks)
#pragma unroll
        for (int nt = 0; nt < 2; ++nt) wf[blk][ks][nt] = wfrag[(((blk * 2 + ks) * 2 + uu) * 2 + nt) * 64 + lane];
  }
  f32x16 ep0 = *(const f32x16*)epsf, ep1 = *(const f32x16*)(epsf + 16);
  asm volatile("" : "+s"(ep0), "+s"(ep1));
  bar();
  TS(1, 6);
  if (tile * 16 < nn) {
    const int node = tile * 16 + (lane & 15), q = lane >> 4;
    const int ni = *LDSP(const int, LB + L_NI + node * 4), se = ni >> 18;
    const u32 sw = (q ^ (node & 7)) * 16, ua = LB + (ni & 0x3ffff), ug = LB + *LDSP(const int, LB + L_NI + 256 + node * 4) - 2048;
    u32x4 uv[16][2];
#pragma unroll
    for (int v = 0; v < 16; ++v) {
      const int vec = (0xedb976c32814fa50ull >> (v * 4)) & 15;
#pragma unroll
      for (int ks = 0; ks < 2; ++ks) uv[vec][ks] = *LDSP(const u32x4, ua + (sw ^ (ks * 64)) + vec * 128);
    }
    __builtin_amdgcn_sched_barrier(0);
    auto mm = [&](int blk, int vec, f32x4 (&ac)[2]) AINL {
#pragma unroll
      for (int ks = 0; ks < 2; ++ks) {
        u32x4 bv = vec < 16 ? uv[vec & 15][ks] : *LDSP(const u32x4, ug + vec * 128 + (sw ^ (ks * 64)));
#pragma unroll
        for (int nt = 0; nt < 2; ++nt)
          ac[nt] = __builtin_amdgcn_mfma_f32_16x16x32_bf16(__builtin_bit_cast(bf16x8, wf[blk][ks][nt]), __builtin_bit_cast(bf16x8, bv), ac[nt], 0, 0, 0);
      }
    };
    const f32x4 z4 = {0, 0, 0, 0};
    f32x4 a0[2] = {z4, z4}, a1[3][2] = {{z4, z4}, {z4, z4}, {z4, z4}};
    mm(0, 0, a0); mm(1, 5, a0); mm(1, 10, a0); mm(1, 15, a0);
#pragma unroll
    for (int i = 0; i < 3; ++i) { mm(2, (i + 1) * 4, a1[i]); mm(3, i + 1, a1[i]); }
#pragma unroll
    for (int j = 0; j < 3; ++j)
#pragma unroll
      for (int k = 0; k < 3; ++k) {
        if (j == k && !gen) continue;
        f32x4 v[2] = {z4, z4};
        mm(4, j != k ? (j + 1) * 4 + k + 1 : j == 0 ? 17 : j == 1 ? 16 : 18, v);
#pragma unroll
        for (int i = 0; i < 3; ++i) {
          const int ei = i * 9 + j * 3 + k;
          float e = ei < 16 ? ep0[ei & 15] : ep1[ei & 15];
          { a1[i][0] += e * v[0]; a1[i][1] += e * v[1]; }
        }
      }
    if (node < nn) {
      const u32 op = (n0 + node) * 512 + uu * 64 + q * 16;
      float g[8];
#pragma unroll
      for (int i = 0; i < 8; ++i) {
        a0[i >> 2][i & 3] = __builtin_amdgcn_ldexpf(a0[i >> 2][i & 3], se);
#pragma unroll
        for (int j = 0; j < 3; ++j) a1[j][i >> 2][i & 3] = __builtin_amdgcn_ldexpf(a1[j][i >> 2][i & 3], se);
      }
#pragma unroll
      for (int i = 0; i < 8; ++i) g[i] = __builtin_amdgcn_rcpf(1.f + __expf(-a0[i >> 2][i & 3]));
      asm volatile(".Lkm_end:");
      store_wt<STO>(out, op, u32x4{cvtpk(a0[0][0], a0[0][1]), cvtpk(a0[0][2], a0[0][3]), cvtpk(a0[1][0], a0[1][1]), cvtpk(a0[1][2], a0[1][3])});
#pragma unroll
      for (int i = 0; i < 3; ++i)
        store_wt<STO>(out, op + 128 + i * 128, u32x4{cvtpk(a1[i][0][0] * g[0], a1[i][0][1] * g[1]), cvtpk(a1[i][0][2] * g[2], a1[i][0][3] * g[3]),
                                            cvtpk(a1[i][1][0] * g[4], a1[i][1][1] * g[5]), cvtpk(a1[i][1][2] * g[6], a1[i][1][3] * g[7])});
    }
  }
  TS(1, 7);
}

extern "C" void kernel_launch(void* const* d_in, const int* in_sizes, int n_in, void* d_out, int out_size, void* d_ws, size_t ws_size,
                              hipStream_t stream) {
  const u16* x = (const u16*)d_in[0]; const u16* rhat = (const u16*)d_in[1]; const u32x4* ea = (const u32x4*)d_in[2];
  const int* src = (const int*)d_in[3]; const int* dst = (const int*)d_in[4];
  const u16* wrad = (const u16*)d_in[5]; const u16* wsi0 = (const u16*)d_in[6]; const u16* wsi1 = (const u16*)d_in[7]; const u16* eps = (const u16*)d_in[8];
  const int N = in_sizes[0] / 256, E = in_sizes[3];
  const int chunk = (E + NCH - 1) / NCH;
  const u32 M = (u32)(((unsigned long long)NBK << 24) / (unsigned long long)N);
  char* ws = (char*)d_ws;
  u32x4* recA = (u32x4*)ws;
  u32* recM = (u32*)ws;
  u16* tab = (u16*)(ws + NCH * CHCAP * 12);
  u16* wfrag = tab + NCH * TABS;
  float* epsf = (float*)(wfrag + 20480);
  u32* wimg = (u32*)(epsf + 32);
  unsigned char* exb = (unsigned char*)(wimg + 2560);
  u32x4* xh = (u32x4*)(ws + (8u << 20));
  const int rpb = (N + NCH - 1) / NCH;
  k_bin<<<NCH + 6, 512, 0, stream>>>(rhat, src, dst, wsi0, wsi1, eps, recA, recM, tab, wfrag, epsf, (const u32x4*)x, (const u32x4*)wrad, xh, exb, wimg,
                                 E, chunk, M, N, rpb);
  k_main<<<NBK, 512, 0, stream>>>(x, wrad, recA, recM, tab, (const u32x4*)wfrag, epsf, (u16*)d_out, (const u16*)xh, (const u32x4*)exb,
                                  (const u32x4*)wimg, ea, (u32x4*)(ws + (16u << 20)), N, M, (E - 1) / 4);
}
```
